```python
import torch

def _softmax(x, axis=-1):
    return torch.softmax(x, dim=axis)
def _gelu(x):
    return torch.nn.functional.gelu(x, approximate="tanh")
def _silu(x):
    return torch.nn.functional.silu(x)
def _sigmoid(x):
    return torch.sigmoid(x)
def _layernorm(x, axis=-1, eps=1e-5):
    m = x.mean(dim=axis, keepdim=True)
    v = x.var(dim=axis, keepdim=True, unbiased=False)
    return (x - m) / torch.sqrt(v + eps)
def _scatter_add(tgt, idx, src, axis=0):
    return tgt.clone().index_add_(axis, idx, src)
def _segment_sum(src, seg, n):
    out = torch.zeros((n,) + src.shape[1:], dtype=src.dtype)
    return out.index_add_(0, seg, src)
def _pad(x, pw):
    flat = tuple(v for pair in reversed(pw) for v in pair)
    return torch.nn.functional.pad(x, flat)

kernel_name = 'gated_mix__spectral_gfnet__glu_gated_geglu__prompt'

def setup_inputs():

    _w = (lambda a: (lambda t: t.to(torch.bfloat16) if t.is_floating_point() else t)(torch.as_tensor(a)))
    def _A_setup():
        B, S, D = 1, 2048, 448
        Df = D // 2 + 1
        return {
            "x": _w((lambda *s: torch.randn(*s, dtype=torch.bfloat16) * 0.05)(B, S, D)),
            "w_r": _w((lambda *s: torch.randn(*s, dtype=torch.bfloat16) * 0.05)(S, Df) * 0.02),
            "w_i": _w((lambda *s: torch.randn(*s, dtype=torch.bfloat16) * 0.05)(S, Df) * 0.02),
        }
    _A_in = _A_setup()

    def _B_setup():
        B, S, Dm, Df = 1, 2048, 448, 1792
        return {
            "x": _w((lambda *s: torch.randn(*s, dtype=torch.bfloat16) * 0.05)(B, S, Dm)),
            "w_gate": _w((lambda *s: torch.randn(*s, dtype=torch.bfloat16) * 0.2)(Dm, Df) * (Dm ** -0.5)),
            "w_up": _w((lambda *s: torch.randn(*s, dtype=torch.bfloat16) * 0.2)(Dm, Df) * (Dm ** -0.5)),
            "w_down": _w((lambda *s: torch.randn(*s, dtype=torch.bfloat16) * 0.2)(Df, Dm) * (Df ** -0.5)),
        }
    _B_in = _B_setup()

    return {
        "A_x": _A_in["x"],
        "A_w_r": _A_in["w_r"],
        "A_w_i": _A_in["w_i"],
        "B_x": _B_in["x"],
        "B_w_gate": _B_in["w_gate"],
        "B_w_up": _B_in["w_up"],
        "B_w_down": _B_in["w_down"],
        "gate_w": _w((lambda *s: torch.randn(*s, dtype=torch.bfloat16) * 0.05)(448) * 16.0),
    }

def reference(A_x, A_w_r, A_w_i, B_x, B_w_gate, B_w_up, B_w_down, gate_w):
    def _A_ref(x, w_r, w_i):
        B, S, D = x.shape
        X = (lambda x, axes=(-2,-1): torch.fft.rfft2(x.float(), dim=axes))(x, axes=(-2, -1))
        W = w_r + 1j * w_i
        X = X * W
        y = (lambda x, s, axes=(-2,-1): torch.fft.irfft2(x, s=s, dim=axes).to(torch.bfloat16))(X, (S, D), axes=(-2, -1))
        return y

    def _B_ref(x, w_gate, w_up, w_down):
        g = torch.einsum("bsd,df->bsf", x, w_gate)
        u = torch.einsum("bsd,df->bsf", x, w_up)
        h = _gelu(g) * u
        return torch.einsum("bsf,fd->bsd", h, w_down)

    x = A_x
    g = _sigmoid((x * gate_w).sum(-1, keepdims=True))
    a = _A_ref(x, A_w_r, A_w_i)
    b = _B_ref(x, B_w_gate, B_w_up, B_w_down)
    return g * a + (1 - g) * b
```

```cpp
#include <hip/hip_runtime.h>
#include <hip/hip_bf16.h>
#include <stdint.h>

#define S_ 2048
#define D_ 448
#define KF 225
#define F_ 1792

__device__ __forceinline__ float bf2f(unsigned short v){ return __uint_as_float(((unsigned)v)<<16); }
__device__ __forceinline__ unsigned short f2bf(float f){ unsigned u=__float_as_uint(f); u += 0x7fffu + ((u>>16)&1u); return (unsigned short)(u>>16); }
__device__ __forceinline__ float rbf(float f){ return bf2f(f2bf(f)); }

__global__ void k_rowdft(const unsigned short* x, float2* X1){
  int s = blockIdx.x; int k = threadIdx.x;
  __shared__ float xs[D_]; __shared__ float2 tw[D_];
  for(int d=threadIdx.x; d<D_; d+=blockDim.x){ xs[d]=bf2f(x[s*D_+d]); float sn,cs; sincospif(-2.0f*d/(float)D_,&sn,&cs); tw[d]=make_float2(cs,sn);}
  __syncthreads();
  if(k<KF){ float re=0,im=0; int idx=0; for(int d=0; d<D_; ++d){ re+=xs[d]*tw[idx].x; im+=xs[d]*tw[idx].y; idx+=k; if(idx>=D_) idx-=D_; } X1[s*KF+k]=make_float2(re,im);}
}
__global__ void k_coldft(const float2* in, float2* out, const unsigned short* wr, const unsigned short* wi, float sign, int mulw){
  int k = blockIdx.x;
  __shared__ float2 col[S_]; __shared__ float2 tw[S_];
  for(int s=threadIdx.x; s<S_; s+=blockDim.x){ col[s]=in[s*KF+k]; float sn,cs; sincospif(sign*2.0f*s/(float)S_,&sn,&cs); tw[s]=make_float2(cs,sn);}
  __syncthreads();
  for(int sp=threadIdx.x; sp<S_; sp+=blockDim.x){
    float re=0,im=0; int idx=0;
    for(int s=0;s<S_;++s){ float2 c=col[s]; float2 t=tw[idx]; re+=c.x*t.x-c.y*t.y; im+=c.x*t.y+c.y*t.x; idx=(idx+sp)&(S_-1);}
    if(mulw){ float a=bf2f(wr[sp*KF+k]), b=bf2f(wi[sp*KF+k]); float r2=re*a-im*b, i2=re*b+im*a; re=r2; im=i2; }
    out[sp*KF+k]=make_float2(re,im);
  }
}
__global__ void k_gemm1(const unsigned short* x, const unsigned short* wg, const unsigned short* wu, unsigned short* h){
  int s = blockIdx.x; __shared__ float xs[D_];
  for(int d=threadIdx.x; d<D_; d+=blockDim.x) xs[d]=bf2f(x[s*D_+d]);
  __syncthreads();
  for(int f=threadIdx.x; f<F_; f+=blockDim.x){
    float g=0,u=0; for(int d=0; d<D_; ++d){ g+=xs[d]*bf2f(wg[d*F_+f]); u+=xs[d]*bf2f(wu[d*F_+f]); }
    g=rbf(g); u=rbf(u);
    float t = tanhf(0.7978845608028654f*(g+0.044715f*g*g*g));
    float ge = rbf(0.5f*g*(1.0f+t));
    h[s*F_+f]=f2bf(ge*u);
  }
}
__global__ void k_final(const unsigned short* x, const unsigned short* gw, const float2* Y, const unsigned short* h, const unsigned short* wd, unsigned short* out){
  int s = blockIdx.x; __shared__ float hs[F_]; __shared__ float2 ys[KF]; __shared__ float2 tw[D_]; __shared__ float red[512];
  for(int f=threadIdx.x; f<F_; f+=blockDim.x) hs[f]=bf2f(h[s*F_+f]);
  for(int k=threadIdx.x; k<KF; k+=blockDim.x) ys[k]=Y[s*KF+k];
  for(int d=threadIdx.x; d<D_; d+=blockDim.x){ float sn,cs; sincospif(2.0f*d/(float)D_,&sn,&cs); tw[d]=make_float2(cs,sn);}
  int d=threadIdx.x;
  red[d] = d<D_ ? rbf(bf2f(x[s*D_+d])*bf2f(gw[d])) : 0.f;
  __syncthreads();
  float gs=0; for(int i=0;i<D_;++i) gs+=red[i];
  gs=rbf(gs); float g = rbf(1.0f/(1.0f+expf(-gs)));
  if(d<D_){
    float a = ys[0].x + ((d&1)? -ys[224].x : ys[224].x);
    int idx=0;
    for(int k=1;k<224;++k){ idx+=d; if(idx>=D_) idx-=D_; a += 2.0f*(ys[k].x*tw[idx].x - ys[k].y*tw[idx].y); }
    a *= 1.0f/(float)(S_*D_);
    a = rbf(a);
    float b=0; for(int f=0; f<F_; ++f) b+=hs[f]*bf2f(wd[f*D_+d]);
    b=rbf(b);
    float o = rbf(g*a) + rbf(rbf(1.0f-g)*b);
    out[s*D_+d]=f2bf(o);
  }
}

extern "C" void kernel_launch(void* const* d_in, const int* in_sizes, int n_in,
                              void* d_out, int out_size, void* d_ws, size_t ws_size,
                              hipStream_t stream) {
  const unsigned short* x=(const unsigned short*)d_in[0];
  const unsigned short* wr=(const unsigned short*)d_in[1];
  const unsigned short* wi=(const unsigned short*)d_in[2];
  const unsigned short* wg=(const unsigned short*)d_in[4];
  const unsigned short* wu=(const unsigned short*)d_in[5];
  const unsigned short* wd=(const unsigned short*)d_in[6];
  const unsigned short* gw=(const unsigned short*)d_in[7];
  char* ws=(char*)d_ws;
  float2* X1=(float2*)ws; float2* X2=(float2*)(ws+(8<<20)); float2* X3=(float2*)(ws+(16<<20)); unsigned short* h=(unsigned short*)(ws+(24<<20));
  k_rowdft<<<S_,256,0,stream>>>(x,X1);
  k_coldft<<<KF,1024,0,stream>>>(X1,X2,wr,wi,-1.0f,1);
  k_coldft<<<KF,1024,0,stream>>>(X2,X3,wr,wi,1.0f,0);
  k_gemm1<<<S_,256,0,stream>>>(x,wg,wu,h);
  k_final<<<S_,512,0,stream>>>(x,gw,X3,h,wd,(unsigned short*)d_out);
}
```

```cpp
#include <hip/hip_runtime.h>
#include <hip/hip_bf16.h>
#include <stdint.h>

#define TSTAMP 0

namespace ct {
constexpr double PI = 3.14159265358979323846;
constexpr double csin(double x) { double x2 = x * x, t = x, s = x; for (int i = 1; i < 10; ++i) { t *= -x2 / ((2 * i) * (2 * i + 1)); s += t; } return s; }
constexpr double ccos(double x) { double x2 = x * x, t = 1, s = 1; for (int i = 1; i < 10; ++i) { t *= -x2 / ((2 * i - 1) * (2 * i)); s += t; } return s; }
constexpr double cos448(int i) {
  if (i > 224) i = 448 - i;
  if (i > 112) return -cos448(224 - i);
  if (i > 56) return csin(2 * PI * (112 - i) / 448.0);
  return ccos(2 * PI * i / 448.0);
}
constexpr unsigned short tobf(double d) { float f = (float)d; unsigned u = __builtin_bit_cast(unsigned, f); u += 0x7fffu + ((u >> 16) & 1u); return (unsigned short)(u >> 16); }
struct C448 { unsigned short v[448]; };
constexpr C448 mk448() { C448 c{}; for (int i = 0; i < 448; ++i) c.v[i] = tobf(cos448(i)); return c; }
constexpr C448 COS = mk448();
struct GPart { unsigned short v[28 * 2 * 64 * 8]; };
constexpr GPart mkG(int ctile) {
  GPart g{};
  for (int kk = 0; kk < 28; ++kk) for (int dt = 0; dt < 2; ++dt) for (int l = 0; l < 64; ++l) {
    const int d = 64 * ctile + 32 * dt + (l & 31); const int h = l >> 5; const int im = kk & 1; const int kb = 16 * (kk >> 1) + 4 * h;
    for (int e = 0; e < 8; ++e) {
      const int k = kb + 8 * (e >> 2) + (e & 3);
      int idx = (d * k + (im ? 112 : 0)) % 448;
      if (im && k == 0) idx = (d & 1) * 224;
      g.v[((kk * 2 + dt) * 64 + l) * 8 + e] = COS.v[idx];
    }
  }
  return g;
}
constexpr GPart G0 = mkG(0), G1 = mkG(1), G2 = mkG(2), G3 = mkG(3), G4 = mkG(4), G5 = mkG(5), G6 = mkG(6);
struct GT { GPart p[7]; };
}
__device__ __attribute__((aligned(256))) const ct::GT GTAB = {{ct::G0, ct::G1, ct::G2, ct::G3, ct::G4, ct::G5, ct::G6}};

namespace gf {
typedef __bf16 bf16x8 __attribute__((ext_vector_type(8)));
typedef __bf16 bf16x4 __attribute__((ext_vector_type(4)));
typedef __bf16 bf16x2 __attribute__((ext_vector_type(2)));
typedef float f32x4 __attribute__((ext_vector_type(4)));
typedef float f32x16 __attribute__((ext_vector_type(16)));
typedef float f32x2 __attribute__((ext_vector_type(2)));
typedef unsigned u32x4 __attribute__((ext_vector_type(4)));
typedef unsigned u32x2 __attribute__((ext_vector_type(2)));
typedef unsigned long long u64;
#define LAS __attribute__((address_space(3)))
#define DI __device__ __forceinline__

constexpr unsigned EP_OFF = 0;
constexpr unsigned F1_OFF = 1024;
constexpr unsigned FW_OFF = 4096;
constexpr unsigned FH_OFF = 8192;
constexpr unsigned FY_OFF = 16384 + 8192;
constexpr unsigned TS_OFF = 32768;
constexpr unsigned X1_OFF = 131072;
constexpr unsigned YT_OFF = X1_OFF + 448 * 4096;
constexpr unsigned WT_OFF = YT_OFF + 448 * 4096;
constexpr unsigned G_OFF  = WT_OFF + 225 * 8192;
constexpr unsigned HF_OFF = G_OFF + 8192;
constexpr unsigned WS_END = HF_OFF + 64u * 56 * 2048;

constexpr int NSLOT = 13;
constexpr int AUX  = NSLOT * 8192;
constexpr int FT   = AUX;
constexpr int T3_X = AUX;
constexpr int T3_Y = T3_X + 10240;
constexpr int T3_C64 = AUX + 28672, T3_S64 = T3_C64 + 8192, T3_C32 = T3_C64 + 16384, T3_S32 = T3_C32 + 2048;
constexpr int LDS_BYTES = 163840;
static_assert(T3_S32 + 2048 <= LDS_BYTES, "lds");

DI unsigned pk2(float a, float b) { f32x2 v = {a, b}; return __builtin_bit_cast(unsigned, __builtin_convertvector(v, bf16x2)); }
DI float rbf(float a) { return __uint_as_float(pk2(a, 0.f) << 16); }
DI float bflo(unsigned u) { return __uint_as_float(u << 16); }
DI float bfhi(unsigned u) { return __uint_as_float(u & 0xffff0000u); }
DI f32x4 mfma(u32x4 a, u32x4 b, f32x4 c) { return __builtin_amdgcn_mfma_f32_16x16x32_bf16(__builtin_bit_cast(bf16x8, a), __builtin_bit_cast(bf16x8, b), c, 0, 0, 0); }
DI f32x16 mfma32(u32x4 a, u32x4 b, f32x16 c) { return __builtin_amdgcn_mfma_f32_32x32x16_bf16(__builtin_bit_cast(bf16x8, a), __builtin_bit_cast(bf16x8, b), c, 0, 0, 0); }
DI u32x2 tr_read(unsigned addr) { return __builtin_bit_cast(u32x2, __builtin_amdgcn_ds_read_tr16_b64_v4bf16((LAS bf16x4*)(size_t)addr)); }
DI u32x2 lds_r64(unsigned addr) { return *(LAS u32x2*)(size_t)addr; }
DI unsigned lds_r32(unsigned addr) { return *(LAS unsigned*)(size_t)addr; }
DI u32x4 lds_r128(unsigned addr) { return *(LAS u32x4*)(size_t)addr; }
DI void lds_w128(unsigned addr, u32x4 v) { *(LAS u32x4*)(size_t)addr = v; }
DI void lds_w64(unsigned addr, u32x2 v) { *(LAS u32x2*)(size_t)addr = v; }
DI void lds_w32(unsigned addr, unsigned v) { *(LAS unsigned*)(size_t)addr = v; }
DI void glds16(const char* sbase, unsigned voff, unsigned ldsdst) {
  asm volatile("s_mov_b32 m0, %2\n\ts_nop 0\n\tglobal_load_lds_dwordx4 %0, %1" :: "v"(voff), "s"(sbase), "s"(ldsdst) : "memory");
}
DI void ld128_sc1(u32x4& d, const char* sbase, unsigned voff) { asm volatile("global_load_dwordx4 %0, %1, %2 sc1" : "=v"(d) : "v"(voff), "s"(sbase) : "memory"); }
template <int IMM> DI void ld128_sc1i(u32x4& d, const char* sbase, unsigned voff) { asm volatile("global_load_dwordx4 %0, %1, %2 offset:%3 sc1" : "=v"(d) : "v"(voff), "s"(sbase), "i"(IMM) : "memory"); }
DI void ld64_sc1(u32x2& d, const char* sbase, unsigned voff) { asm volatile("global_load_dwordx2 %0, %1, %2 sc1" : "=v"(d) : "v"(voff), "s"(sbase) : "memory"); }
DI void ld32_sc1(unsigned& d, const char* sbase, unsigned voff) { asm volatile("global_load_dword %0, %1, %2 sc1" : "=v"(d) : "v"(voff), "s"(sbase) : "memory"); }
#define WAITV(n) asm volatile("s_waitcnt vmcnt(%0)" :: "i"(n) : "memory")
#define TIE(x) asm volatile("" : "+v"(x))
#define WAITL0() asm volatile("s_waitcnt lgkmcnt(0)" ::: "memory")
#define BAR() do { asm volatile("" ::: "memory"); __builtin_amdgcn_s_barrier(); asm volatile("" ::: "memory"); } while (0)
#define LBAR() do { WAITL0(); BAR(); } while (0)
DI u32x4 cat(u32x2 a, u32x2 b) { return u32x4{a.x, a.y, b.x, b.y}; }
DI float cosr(float r) { return __builtin_amdgcn_cosf(r); }
DI float sinr(float r) { return __builtin_amdgcn_sinf(r); }
DI u32x4 neg8(u32x4 v) { return v ^ u32x4{0x80008000u, 0x80008000u, 0x80008000u, 0x80008000u}; }
typedef __amdgpu_buffer_rsrc_t rsrc_t;
DI void st128(rsrc_t rs, unsigned off, u32x4 v) { __builtin_amdgcn_raw_buffer_store_b128(v, rs, off, 0, 16); }
DI void st64(rsrc_t rs, unsigned off, u32x2 v) { __builtin_amdgcn_raw_buffer_store_b64(v, rs, off, 0, 16); }
DI void st32(rsrc_t rs, unsigned off, unsigned v) { __builtin_amdgcn_raw_buffer_store_b32(v, rs, off, 0, 16); }
constexpr int SPIN_CAP = 1 << 18;
DI void spin_flags(u32x2 f, u32x2 tag, const char* ws, unsigned voff) {
  int spins = 0;
  while (__builtin_amdgcn_ballot_w64(f.x != tag.x || f.y != tag.y) != 0ull) {
    if (++spins > SPIN_CAP) break;
    __builtin_amdgcn_s_sleep(1);
    ld64_sc1(f, ws, voff); WAITV(0); TIE(f);
  }
}
#define STAMP(i)

DI void t3_consts() {
  const int tid = threadIdx.x;
  { int fr = tid >> 6, ll = tid & 63; int rt = fr >> 1, ks = fr & 1; int i = 16 * rt + (ll & 15); int k0 = 32 * ks + 8 * (ll >> 4);
    float c[8], s[8];
#pragma unroll
    for (int e = 0; e < 8; ++e) { float a = (float)(i * (k0 + e)) * (1.0f / 64); c[e] = cosr(a); s[e] = sinr(a); }
    lds_w128(T3_C64 + tid * 16, u32x4{pk2(c[0], c[1]), pk2(c[2], c[3]), pk2(c[4], c[5]), pk2(c[6], c[7])});
    lds_w128(T3_S64 + tid * 16, u32x4{pk2(s[0], s[1]), pk2(s[2], s[3]), pk2(s[4], s[5]), pk2(s[6], s[7])});
  }
  {
    int t = tid >> 8, ll = (tid >> 2) & 63, e2 = tid & 3; int i = 16 * t + (ll & 15); int QQ = ll >> 4; int k = 16 * (QQ >> 1) + 8 * (e2 >> 1) + 4 * (QQ & 1) + 2 * (e2 & 1);
    float a0 = (float)(i * k) * (1.0f / 32), a1 = (float)(i * (k + 1)) * (1.0f / 32);
    lds_w32(T3_C32 + tid * 4, pk2(cosr(a0), cosr(a1)));
    lds_w32(T3_S32 + tid * 4, pk2(sinr(a0), sinr(a1)));
  }
}

struct Tw { float fc[4], fs[4], ic[4], is[4]; };

DI void t3(int nre, bool with_im, u32x4 zr, u32x4 zi, u32x4 wv, const Tw& tw, rsrc_t rs) {
  int tid = threadIdx.x; TIE(tid);
  const int l = tid & 63;
  const int w = __builtin_amdgcn_readfirstlane(tid >> 6);
  const int t4 = w >> 1, t2 = w & 1;
  const int r = l & 15, Q = l >> 4;
  const f32x4 zero = {0, 0, 0, 0};
  const unsigned c64a = T3_C64 + t4 * 2048 + l * 16;
  const unsigned c32a = T3_C32 + t2 * 1024 + l * 16;
  const int ap = 16 * t2 + r;
  const unsigned rowa = ap * 144;
  {
    const u32x4 C32 = lds_r128(c32a), S32 = lds_r128(c32a + 2048);
    f32x4 re = mfma(zr, C32, zero); re = mfma(zi, S32, re);
    f32x4 im = mfma(zi, C32, zero); im = mfma(neg8(zr), S32, im);
    float tr[4], ti[4];
#pragma unroll
    for (int i = 0; i < 4; ++i) { tr[i] = re[i] * tw.fc[i] + im[i] * tw.fs[i]; ti[i] = im[i] * tw.fc[i] - re[i] * tw.fs[i]; }
    unsigned o = T3_X + rowa + (16 * t4 + 4 * Q) * 2;
    lds_w64(o, u32x2{pk2(tr[0], tr[1]), pk2(tr[2], tr[3])});
    lds_w64(o + 32 * 144, u32x2{pk2(ti[0], ti[1]), pk2(ti[2], ti[3])});
  }
  u32x4 C64[2], S64[2];
#pragma unroll
  for (int ks = 0; ks < 2; ++ks) { C64[ks] = lds_r128(c64a + ks * 1024); S64[ks] = lds_r128(c64a + 8192 + ks * 1024); }
  LBAR();
  {
    f32x4 re = zero, im = zero;
#pragma unroll
    for (int ks = 0; ks < 2; ++ks) {
      unsigned o = T3_X + rowa + (32 * ks + 8 * Q) * 2;
      u32x4 dr = lds_r128(o), di = lds_r128(o + 32 * 144);
      re = mfma(C64[ks], dr, re); re = mfma(S64[ks], di, re);
      im = mfma(C64[ks], di, im); im = mfma(S64[ks], neg8(dr), im);
    }
    float vr[4], vi[4];
#pragma unroll
    for (int i = 0; i < 4; ++i) { unsigned wq = wv[i]; float a = bflo(wq), b = bfhi(wq);
      vr[i] = re[i] * a - im[i] * b; vi[i] = re[i] * b + im[i] * a; }
    unsigned o = T3_Y + rowa + (16 * t4 + 4 * Q) * 2;
    lds_w64(o, u32x2{pk2(vr[0], vr[1]), pk2(vr[2], vr[3])});
    lds_w64(o + 32 * 144, u32x2{pk2(vi[0], vi[1]), pk2(vi[2], vi[3])});
  }
  LBAR();
  const int b = 16 * t4 + r;
  {
    f32x4 re = zero, im = zero;
#pragma unroll
    for (int ks = 0; ks < 2; ++ks) {
      unsigned o = T3_Y + rowa + (32 * ks + 8 * Q) * 2;
      u32x4 dr = lds_r128(o), di = lds_r128(o + 32 * 144);
      re = mfma(dr, C64[ks], re); re = mfma(neg8(di), S64[ks], re);
      im = mfma(di, C64[ks], im); im = mfma(dr, S64[ks], im);
    }
    float tr[4], ti[4];
#pragma unroll
    for (int i = 0; i < 4; ++i) { tr[i] = re[i] * tw.ic[i] - im[i] * tw.is[i]; ti[i] = im[i] * tw.ic[i] + re[i] * tw.is[i]; }
    unsigned o = T3_X + b * 80 + (16 * t2 + 4 * Q) * 2;
    lds_w64(o, u32x2{pk2(tr[0], tr[1]), pk2(tr[2], tr[3])});
    lds_w64(o + 64 * 80, u32x2{pk2(ti[0], ti[1]), pk2(ti[2], ti[3])});
  }
  const u32x4 C32 = lds_r128(c32a), S32 = lds_r128(c32a + 2048);
  LBAR();
  {
    unsigned o = T3_X + b * 80 + (Q >> 1) * 32 + (Q & 1) * 8;
    u32x4 dr = cat(lds_r64(o), lds_r64(o + 16)), di = cat(lds_r64(o + 64 * 80), lds_r64(o + 64 * 80 + 16));
    f32x4 re = mfma(C32, dr, zero); re = mfma(S32, neg8(di), re);
    f32x4 im = mfma(C32, di, zero); im = mfma(S32, dr, im);
    unsigned oo = T3_Y + b * 64 + (16 * t2 + 4 * Q) * 2;
    lds_w64(oo, u32x2{pk2(re[0], re[1]), pk2(re[2], re[3])});
    lds_w64(oo + 4096, u32x2{pk2(im[0], im[1]), pk2(im[2], im[3])});
  }
  LBAR();
  if (with_im || w < 4) st128(rs, YT_OFF + (nre + 16 * (tid >> 8)) * 4096 + (tid & 255) * 16, lds_r128(T3_Y + tid * 16));
}

DI void make_tw(Tw& tw, float sc) {
  const int tid = threadIdx.x, l = tid & 63, w = tid >> 6;
  const int t4 = w >> 1, t2 = w & 1, r = l & 15, Q = l >> 4;
#pragma unroll
  for (int i = 0; i < 4; ++i) {
    float a = (float)((16 * t4 + 4 * Q + i) * (16 * t2 + r)) * (1.0f / 2048); tw.fc[i] = cosr(a); tw.fs[i] = sinr(a);
    float b = (float)((16 * t4 + r) * (16 * t2 + 4 * Q + i)) * (1.0f / 2048); tw.ic[i] = cosr(b) * sc; tw.is[i] = sinr(b) * sc;
    TIE(tw.fc[i]); TIE(tw.fs[i]); TIE(tw.ic[i]); TIE(tw.is[i]);
  }
}

DI void helper(int hw, const unsigned short* x, const unsigned short* wr, const unsigned short* wi, const unsigned short* gwp, char* ws, rsrc_t rs) {
  const int bt = hw >> 3, q = (hw >> 1) & 3, at = hw & 1;
  const int tid = threadIdx.x, l = tid & 63, w = tid >> 6;
  u32x2 tag; ld64_sc1(tag, ws, EP_OFF);
  u32x4 wv[8];
  const int tq = tid < 450 ? tid : 449;
#pragma unroll
  for (int i = 0; i < 4; ++i) {
    const int row0 = 16 * at + 32 * (16 * bt + 4 * q + i);
    wv[2 * i] = *(const u32x4*)(wr + row0 * 225 + tq * 8);
    wv[2 * i + 1] = *(const u32x4*)(wi + row0 * 225 + tq * 8);
  }
  const int lq = l < 56 ? l : 55;
  u32x4 gv = *(const u32x4*)(gwp + lq * 8);
  u32x4 xv[8];
#pragma unroll
  for (int i = 0; i < 8; ++i) xv[i] = *(const u32x4*)(x + (size_t)(64 * hw + 8 * w + i) * 448 + lq * 8);
#pragma unroll
  for (int i = 0; i < 8; ++i) lds_w128(i * 7232 + tq * 16, wv[i]);
  float gs = 0.f;
#pragma unroll
  for (int i = 0; i < 8; ++i) {
    float t = 0.f;
#pragma unroll
    for (int e = 0; e < 4; ++e) { unsigned xe = xv[i][e], ge = gv[e]; unsigned pp = pk2(bflo(xe) * bflo(ge), bfhi(xe) * bfhi(ge)); t += bflo(pp) + bfhi(pp); }
    if (l >= 56) t = 0.f;
#pragma unroll
    for (int o = 32; o >= 1; o >>= 1) t += __shfl_xor(t, o);
    if (l == i) gs = t;
  }
  { float t = rbf(gs); float g = rbf(1.0f / (1.0f + __expf(-t))); const int s = 64 * hw + 8 * w + l;
    if (l < 8) st32(rs, G_OFF + ((s & 63) * 32 + (s >> 6)) * 4, __float_as_uint(g)); }
  LBAR();
  LAS unsigned short* L = (LAS unsigned short*)(size_t)0;
#pragma unroll 2
  for (int e = tid; e < 225 * 16; e += 512) {
    int k = e >> 4, r = e & 15;
    u32x4 v;
#pragma unroll
    for (int i = 0; i < 4; ++i) v[i] = (unsigned)L[(2 * i) * 3616 + r * 225 + k] | ((unsigned)L[(2 * i + 1) * 3616 + r * 225 + k] << 16);
    st128(rs, WT_OFF + k * 8192 + (bt * 2 + at) * 1024 + (q * 16 + r) * 16, v);
  }
  WAITV(0); TIE(tag);
  BAR();
  { u64 t = (((u64)tag.y << 32) | tag.x) + 1; tag = u32x2{(unsigned)t, (unsigned)(t >> 32)}; }
  if (tid == 0) st64(rs, FW_OFF + hw * 8, tag);
  if (hw < 2) {
    t3_consts();
    Tw tw; make_tw(tw, 1.0f / (2048.0f * 448.0f));
    const unsigned po = l < 16 ? F1_OFF + l * 8 : FW_OFF + (l & 31) * 8;
    u32x2 f; ld64_sc1(f, ws, po); WAITV(0); TIE(f);
    spin_flags(f, tag, ws, po);
    u32x4 zr, wq;
    ld128_sc1(zr, ws, X1_OFF + hw * 16 * 4096 + (16 * (w >> 1) + (l & 15)) * 64 + (l >> 4) * 16);
    ld128_sc1(wq, ws, WT_OFF + hw * 224 * 8192 + tid * 16);
    WAITV(0); TIE(zr); TIE(wq);
    LBAR();
    t3(hw * 16, false, zr, u32x4{0, 0, 0, 0}, wq, tw, rs);
    WAITV(0);
    BAR();
    if (tid < (hw ? 4 : 1)) st64(rs, FY_OFF + hw * 224 * 8 + tid * 8, tag);
  }
}

__global__ __launch_bounds__(512, 2) void fused(const unsigned short* __restrict__ x, const unsigned short* __restrict__ wr, const unsigned short* __restrict__ wi,
                                                const unsigned short* __restrict__ wg, const unsigned short* __restrict__ wu, const unsigned short* __restrict__ wd,
                                                const unsigned short* __restrict__ gwp, char* ws, unsigned short* __restrict__ out) {
  extern __shared__ __attribute__((aligned(16))) char smem[];
  const rsrc_t rs = __builtin_amdgcn_make_buffer_rsrc(ws, 0, WS_END, 0x00020000);
  const int xcd = blockIdx.x & 7, li = blockIdx.x >> 3;
  STAMP(0);
  if (li >= 28) { helper(xcd * 4 + (li - 28), x, wr, wi, gwp, ws, rs); STAMP(1); return; }
  const int lq = li / 7, lr = li - lq * 7;
  const int p = 4 * (xcd >> 1) + lq, j = 7 * (xcd & 1) + lr;
  const int bb = 4 * xcd + lq, ctile = lr;
  const int kcol = xcd * 28 + li;
  const int tid = threadIdx.x, l = tid & 63;
  const int w = __builtin_amdgcn_readfirstlane(tid >> 6);
  const int m = w & 3, n = w >> 2;
  const int a31 = l & 31, h = l >> 5;
  const unsigned ldw = w * 1024;

  unsigned xoff;
  { int rho = 8 * w + (l >> 3); int mm = rho >> 5, a = rho & 31; int ch = (l & 7) ^ ((rho >> 1) & 7);
    xoff = (64 * a + 4 * p + mm) * 896 + ch * 16; }
#pragma unroll
  for (int u = 0; u < 13; ++u) glds16((const char*)x + (u >> 1) * 128 + (u & 1) * 1792, xoff, u * 8192 + ldw);
  u32x2 tag; ld64_sc1(tag, ws, EP_OFF);
  const unsigned woff = (16 * m + (l >> 2)) * 3584 + 256 * j + (((l >> 1) & 1) ^ n) * 32 + (l & 1) * 16;
  const char* wbase = (const char*)(n ? wu : wg);
  unsigned doff;
  { int rr = 8 * w + (l >> 3); int c2 = ((l >> 2) & 1) ^ ((rr >> 1) & 1);
    doff = ((rr >> 5) * 128 + (rr & 31)) * 896 + ctile * 128 + c2 * 64 + (l & 3) * 16; }
#pragma unroll 1
  for (int it = tid; it < 28 * 32; it += 512) {
    int kk = it >> 5, hh = (it >> 4) & 1, c = it & 15; int kf = 16 * j + c;
    float cs[8], sn[8];
    float rc = cosr(kf * (1.0f / 448)), rsn = sinr(kf * (1.0f / 448));
    int d0 = 16 * kk + 8 * hh; int idx = (d0 * kf) % 448;
    float c0 = cosr(idx * (1.0f / 448)), s0 = -sinr(idx * (1.0f / 448));
#pragma unroll
    for (int i = 0; i < 8; ++i) { cs[i] = c0; sn[i] = s0; float c1 = c0 * rc + s0 * rsn, s1 = s0 * rc - c0 * rsn; c0 = c1; s0 = s1; }
    u32x4 sv = {pk2(sn[0], sn[1]), pk2(sn[2], sn[3]), pk2(sn[4], sn[5]), pk2(sn[6], sn[7])};
    if (kf == 0) sv = u32x4{0xbf803f80u, 0xbf803f80u, 0xbf803f80u, 0xbf803f80u};
    lds_w128(FT + kk * 1024 + (hh * 32 + c) * 16, u32x4{pk2(cs[0], cs[1]), pk2(cs[2], cs[3]), pk2(cs[4], cs[5]), pk2(cs[6], cs[7])});
    lds_w128(FT + kk * 1024 + (hh * 32 + 16 + c) * 16, sv);
  }
  t3_consts();
  Tw tw; make_tw(tw, 2.0f / (2048.0f * 448.0f));
  WAITL0();
  STAMP(1);
  u32x4 xf[28];
  {
    f32x16 accf = {};
    unsigned xa[4];
#pragma unroll
    for (int k4 = 0; k4 < 4; ++k4) xa[k4] = ((m & 1) * 32 + a31) * 128 + (((2 * k4 + h) ^ ((a31 >> 1) & 7)) << 4);
#pragma unroll
    for (int c = 0; c < 7; ++c) {
      WAITV(c == 0 ? 12 : (c == 1 ? 10 : 9));
      BAR();
      if (c == 0) STAMP(2);
      if (c >= 1) {
#pragma unroll
        for (int e = 0; e < 2; ++e) {
          const int u = 11 + 2 * c + e;
          if (u == 13) glds16((const char*)x + 6 * 128 + 1792, xoff, (u % NSLOT) * 8192 + ldw);
          else { const int v = u - 14; glds16(wbase + (v % 7) * 229376 + (v / 7) * 64, woff, (u % NSLOT) * 8192 + ldw); }
        }
      }
      const int s0 = (2 * c) % NSLOT, s1 = (2 * c + 1) % NSLOT;
      const unsigned sb = (m >> 1) ? s1 * 8192 : s0 * 8192;
#pragma unroll
      for (int k4 = 0; k4 < 4; ++k4) xf[4 * c + k4] = lds_r128(sb + xa[k4]);
      if (n == 0) {
#pragma unroll
        for (int k4 = 0; k4 < 4; ++k4) accf = mfma32(xf[4 * c + k4], lds_r128(FT + (4 * c + k4) * 1024 + l * 16), accf);
      }
      WAITL0();
    }
    TIE(tag);
    { u64 t = (((u64)tag.y << 32) | tag.x) + 1; tag = u32x2{(unsigned)t, (unsigned)(t >> 32)}; }
    if (n == 0) {
      unsigned pc[8];
#pragma unroll
      for (int e = 0; e < 8; ++e) pc[e] = pk2(accf[2 * e], accf[2 * e + 1]);
#pragma unroll
      for (int i = 0; i < 2; ++i) {
        const int src = ((l & 1) * 32 + 16 * i + (l >> 2)) * 4;
        u32x4 o;
#pragma unroll
        for (int e = 0; e < 4; ++e) { unsigned lo = __builtin_amdgcn_ds_bpermute(src, pc[e]), hi = __builtin_amdgcn_ds_bpermute(src, pc[4 + e]); o[e] = (l & 2) ? hi : lo; }
        st128(rs, X1_OFF + (32 * j + 16 * i + (l >> 2)) * 4096 + (4 * p + m) * 64 + (l & 3) * 16, o);
      }
    }
  }
  STAMP(3);
  unsigned wsl = (25 % NSLOT) * 8192 + ldw;
  {
    const int g2 = (l >> 4) & 1;
    const unsigned wa = g2 * 4096 + (8 * h + ((l & 15) >> 2)) * 64 + ((n ^ g2) * 32) + (l & 3) * 8;
    unsigned rsl = (14 % NSLOT) * 8192;
    f32x16 acc = {};
    u32x4 B0, B1;
    u32x2 pf; u32x4 zr, zi, wv;
    const unsigned t3poll = l < 16 ? F1_OFF + ((kcol >> 4) * 16 + l) * 8 : FW_OFF + (l & 31) * 8;
    const unsigned hst = HF_OFF + (((4 * p + m) * 56 + j) * 2 + n) * 1024 + l * 16;
    auto epilogue = [&](int su) {
      float hv[8];
#pragma unroll
      for (int e = 0; e < 8; ++e) {
        float g = acc[e], u = acc[8 + e];
        float z = g * (-2.302208198f - 0.1029432397f * g * g);
        float sg = __builtin_amdgcn_rcpf(1.0f + __builtin_amdgcn_exp2f(z));
        hv[e] = g * sg * u;
      }
      st128(rs, hst + su * 28672, u32x4{pk2(hv[0], hv[1]), pk2(hv[2], hv[3]), pk2(hv[4], hv[5]), pk2(hv[6], hv[7])});
    };
    auto issue = [&](const char* cb, unsigned vo, int sub) {
      glds16(cb + sub * 229376, vo, wsl);
      wsl += 8192; if (wsl >= NSLOT * 8192) wsl -= NSLOT * 8192;
    };
#pragma unroll 1
    for (int su = 0; su < 4; ++su) {
      const char* cbA = su + 1 < 4 ? wbase + (su + 1) * 64 : (const char*)wd + (su + 1 - 4) * 28672;
      const char* cbB = su + 2 < 4 ? wbase + (su + 2) * 64 : (const char*)wd + (su + 2 - 4) * 28672;
      const unsigned voA = su + 1 < 4 ? woff : doff, voB = su + 2 < 4 ? woff : doff;
#pragma unroll
      for (int kc = 0; kc < 7; ++kc) {
        if (kc == 0) { if (su > 0) WAITV(6); else WAITV(10); } else WAITV(10);
        BAR();
        if (kc == 0 && su >= 1 && tid == 0)
          st64(rs, su == 1 ? F1_OFF + (j * 16 + p) * 8 : FH_OFF + ((p * 4 + su - 2) * 16 + j) * 8, tag);
        if (kc == 0) { issue(cbA, voA, 4); if (su == 0) issue(cbA, voA, 5); }
        else if (kc == 1) { if (su > 0) issue(cbA, voA, 5); }
        else if (kc == 2) issue(cbA, voA, 6);
        else issue(cbB, voB, kc - 3);
        const unsigned base = rsl + wa;
        rsl += 8192; if (rsl >= NSLOT * 8192) rsl -= NSLOT * 8192;
        u32x4 A0 = cat(tr_read(base), tr_read(base + 256));
        u32x4 A1 = cat(tr_read(base + 1024), tr_read(base + 1024 + 256));
        if (kc > 0) { acc = mfma32(B0, xf[4 * kc - 2], acc); acc = mfma32(B1, xf[4 * kc - 1], acc); }
        else if (su > 0) { acc = mfma32(B0, xf[26], acc); acc = mfma32(B1, xf[27], acc); epilogue(su - 1); acc = f32x16{}; }
        B0 = cat(tr_read(base + 2048), tr_read(base + 2048 + 256));
        B1 = cat(tr_read(base + 3072), tr_read(base + 3072 + 256));
        acc = mfma32(A0, xf[4 * kc], acc); acc = mfma32(A1, xf[4 * kc + 1], acc);
        if (kc == 5 && su == 1) ld64_sc1(pf, ws, t3poll);
        if (kc == 2 && su == 2) {
          WAITV(5); TIE(pf);
          STAMP(4);
          spin_flags(pf, tag, ws, t3poll);
          STAMP(5);
          const int nre = 32 * (kcol >> 4) + (kcol & 15);
          const unsigned xo = X1_OFF + nre * 4096 + (16 * (w >> 1) + (l & 15)) * 64 + (l >> 4) * 16;
          ld128_sc1(zr, ws, xo); ld128_sc1(zi, ws, xo + 16 * 4096); ld128_sc1(wv, ws, WT_OFF + kcol * 8192 + tid * 16);
        }
        if (kc == 1 && su == 3) {
          WAITV(7); TIE(zr); TIE(zi); TIE(wv);
          STAMP(6);
          if (kcol != 0) t3(32 * (kcol >> 4) + (kcol & 15), true, zr, zi, wv, tw, rs);
          STAMP(7);
        }
      }
    }
    acc = mfma32(B0, xf[26], acc); acc = mfma32(B1, xf[27], acc);
    epilogue(3);
  }
  STAMP(8);
  {
    const int mh = w & 1, kq = w >> 1;
    const int pp = bb >> 1;
    const unsigned hpoll = FH_OFF + ((pp * 4 + (l >> 4)) * 16 + ((l & 15) < 14 ? (l & 15) : 0)) * 8;
    const unsigned q2 = (l & 15) >> 2;
    const unsigned ra = (16 * kq + 4 * h + q2) * 128 + ((l >> 4) & 1) * 32 + (l & 3) * 8;
    const unsigned ra0 = ra + ((q2 >> 1) << 6), ra1 = ra + (((q2 >> 1) ^ 1) << 6);
    const unsigned hb = HF_OFF + ((2 * bb + mh) * 112 + kq) * 1024 + l * 16;
    unsigned rsl = ((14 + 28) % NSLOT) * 8192;
    f32x16 acc0 = {}, acc1 = {}, aca0 = {}, aca1 = {};
    u32x4 hf[14];
    auto issue = [&](int d) {
      glds16((const char*)wd + (d / 7) * 28672 + (d % 7) * 229376, doff, wsl);
      wsl += 8192; if (wsl >= NSLOT * 8192) wsl -= NSLOT * 8192;
    };
    auto round = [&](u32x4 hh) {
      u32x4 f0 = cat(tr_read(rsl + ra0), tr_read(rsl + ra0 + 1024));
      u32x4 f1 = cat(tr_read(rsl + ra1), tr_read(rsl + ra1 + 1024));
      rsl += 8192; if (rsl >= NSLOT * 8192) rsl -= NSLOT * 8192;
      acc0 = mfma32(f0, hh, acc0); acc1 = mfma32(f1, hh, acc1);
    };
    u32x2 f;
    ld64_sc1(f, ws, l < 32 ? hpoll : FH_OFF + pp * 512);
    WAITV(0); TIE(f);
    BAR();
    if (tid == 0) { st64(rs, FH_OFF + ((p * 4 + 2) * 16 + j) * 8, tag); st64(rs, FH_OFF + ((p * 4 + 3) * 16 + j) * 8, tag); if (kcol != 0) st64(rs, FY_OFF + kcol * 8, tag); }
    issue(11); issue(12);
#pragma unroll
    for (int u = 0; u < 7; ++u) glds16((const char*)&GTAB + ctile * 57344 + u * 8192, tid * 16, AUX + u * 8192 + ldw);
    spin_flags(f, tag, ws, l < 32 ? hpoll : FH_OFF + pp * 512);
    STAMP(9);
#pragma unroll
    for (int i = 0; i < 14; ++i) ld128_sc1(hf[i], ws, hb + i * 4096);
#pragma unroll
    for (int i = 0; i < 7; ++i) { WAITV(13 - i); TIE(hf[i]); round(hf[i]); }
    LBAR();
#pragma unroll
    for (int d = 13; d < 20; ++d) issue(d);
    ld64_sc1(f, ws, l >= 32 ? hpoll : FH_OFF + pp * 512 + 256);
#pragma unroll
    for (int i = 7; i < 13; ++i) { WAITV(8 + 13 - i); TIE(hf[i]); round(hf[i]); }
    STAMP(10);
    WAITV(0); TIE(f); TIE(hf[13]);
    LBAR();
#pragma unroll
    for (int d = 20; d < 26; ++d) issue(d);
    round(hf[13]);
    spin_flags(f, tag, ws, l >= 32 ? hpoll : FH_OFF + pp * 512 + 256);
    STAMP(11);
#pragma unroll
    for (int i = 0; i < 14; ++i) ld128_sc1(hf[i], ws, hb + (14 + i) * 4096);
    u32x4 f4[2];
    const unsigned po = FY_OFF + (l < 56 ? l : 56) * 32;
    ld128_sc1(f4[0], ws, po); ld128_sc1(f4[1], ws, po + 16);
#pragma unroll
    for (int i = 0; i < 6; ++i) { WAITV(15 - i); TIE(hf[i]); round(hf[i]); }
    LBAR();
    issue(26); issue(27);
#pragma unroll
    for (int i = 6; i < 12; ++i) { WAITV(17 - i); TIE(hf[i]); round(hf[i]); }
    STAMP(12);
    WAITV(0); TIE(f4[0]); TIE(f4[1]); TIE(hf[12]); TIE(hf[13]);
    {
      int spins = 0;
      for (;;) {
        bool bad = f4[0].x != tag.x || f4[0].y != tag.y || f4[0].z != tag.x || f4[0].w != tag.y || f4[1].x != tag.x || f4[1].y != tag.y || f4[1].z != tag.x || f4[1].w != tag.y;
        if (__builtin_amdgcn_ballot_w64(bad) == 0ull || ++spins > SPIN_CAP) break;
        __builtin_amdgcn_s_sleep(1);
        ld128_sc1(f4[0], ws, po); ld128_sc1(f4[1], ws, po + 16); WAITV(0); TIE(f4[0]); TIE(f4[1]);
      }
    }
    STAMP(13);
    if (blockIdx.x == 0 && tid == 0) st64(rs, EP_OFF, tag);
    u32x4 yv[7]; unsigned gq;
    { int rr = 8 * w + (l >> 3); int c2 = ((l >> 2) & 1) ^ ((rr >> 1) & 1);
      const unsigned yoff = YT_OFF + rr * 4096 + bb * 128 + c2 * 64 + (l & 3) * 16;
#pragma unroll
      for (int u = 0; u < 7; ++u) ld128_sc1(yv[u], ws, yoff + u * (64 * 4096)); }
    ld32_sc1(gq, ws, G_OFF + ((2 * bb + mh) * 32 + a31) * 4);
    LBAR();
    round(hf[12]); round(hf[13]);
    unsigned ysl = wsl;
#pragma unroll
    for (int u = 0; u < 7; ++u) { WAITV(7 - u); TIE(yv[u]); lds_w128(wsl + l * 16, yv[u]); wsl += 8192; if (wsl >= NSLOT * 8192) wsl -= NSLOT * 8192; }
    (void)ysl;
    WAITV(0); TIE(gq);
    LBAR();
    STAMP(14);
    {
      const unsigned ya = mh ? ra1 : ra0;
#pragma unroll
      for (int u = 0; u < 7; ++u) {
        u32x4 yf = cat(tr_read(rsl + ya), tr_read(rsl + ya + 1024));
        rsl += 8192; if (rsl >= NSLOT * 8192) rsl -= NSLOT * 8192;
        const unsigned ga = AUX + u * 8192 + kq * 2048 + l * 16;
        aca0 = mfma32(lds_r128(ga), yf, aca0); aca1 = mfma32(lds_r128(ga + 1024), yf, aca1);
      }
    }
    LBAR();
    STAMP(15);
    {
      const float g = __uint_as_float(gq), og = rbf(1.0f - g);
      const unsigned wb = kq * 17408 + (mh * 32 + a31) * 272 + h * 16;
#pragma unroll
      for (int q = 0; q < 4; ++q) {
        f32x4 v0, v1;
#pragma unroll
        for (int e = 0; e < 4; ++e) { v0[e] = aca0[4 * q + e] * g + acc0[4 * q + e] * og; v1[e] = aca1[4 * q + e] * g + acc1[4 * q + e] * og; }
        lds_w128(wb + q * 32, __builtin_bit_cast(u32x4, v0));
        lds_w128(wb + 128 + q * 32, __builtin_bit_cast(u32x4, v1));
      }
    }
    LBAR();
    {
      const int row = tid >> 3, pc = tid & 7;
      f32x4 s0 = {0, 0, 0, 0}, s1 = {0, 0, 0, 0};
#pragma unroll
      for (int q = 0; q < 4; ++q) { unsigned a = q * 17408 + row * 272 + pc * 32; s0 += __builtin_bit_cast(f32x4, lds_r128(a)); s1 += __builtin_bit_cast(f32x4, lds_r128(a + 16)); }
      const int b = 2 * bb + (row >> 5), a = row & 31; const int s = 64 * a + b;
      *(u32x4*)(out + (size_t)s * 448 + ctile * 64 + pc * 8) = u32x4{pk2(s0[0], s0[1]), pk2(s0[2], s0[3]), pk2(s1[0], s1[1]), pk2(s1[2], s1[3])};
    }
    STAMP(16);
  }
}
}

extern "C" void kernel_launch(void* const* d_in, const int* in_sizes, int n_in, void* d_out, int out_size, void* d_ws, size_t ws_size, hipStream_t stream) {
  using namespace gf;
  (void)hipFuncSetAttribute((const void*)fused, hipFuncAttributeMaxDynamicSharedMemorySize, LDS_BYTES);
  fused<<<256, 512, LDS_BYTES, stream>>>((const unsigned short*)d_in[0], (const unsigned short*)d_in[1], (const unsigned short*)d_in[2],
                                         (const unsigned short*)d_in[4], (const unsigned short*)d_in[5], (const unsigned short*)d_in[6],
                                         (const unsigned short*)d_in[7], (char*)d_ws, (unsigned short*)d_out);
}
```

```cpp
#include <hip/hip_runtime.h>
#include <hip/hip_bf16.h>
#include <stdint.h>

#define CODEPF 24576u
#define CPFROM 224u

namespace ct {
constexpr double PI = 3.14159265358979323846;
constexpr double csin(double x) { double x2 = x * x, t = x, s = x; for (int i = 1; i < 10; ++i) { t *= -x2 / ((2 * i) * (2 * i + 1)); s += t; } return s; }
constexpr double ccos(double x) { double x2 = x * x, t = 1, s = 1; for (int i = 1; i < 10; ++i) { t *= -x2 / ((2 * i - 1) * (2 * i)); s += t; } return s; }
constexpr double cosN(int i, int N) {
  if (i > N / 2) i = N - i;
  if (i > N / 4) return -cosN(N / 2 - i, N);
  if (i > N / 8) return csin(2 * PI * (N / 4 - i) / N);
  return ccos(2 * PI * i / N);
}
constexpr unsigned short tobf(double d) { float f = (float)d; unsigned u = __builtin_bit_cast(unsigned, f); u += 0x7fffu + ((u >> 16) & 1u); return (unsigned short)(u >> 16); }
struct C448 { unsigned short v[448]; };
constexpr C448 mk448() { C448 c{}; for (int i = 0; i < 448; ++i) c.v[i] = tobf(cosN(i, 448)); return c; }
constexpr C448 COS = mk448();
struct Part { unsigned short v[28672]; };
constexpr Part mkG(int ctile) {
  Part g{};
  for (int kk = 0; kk < 28; ++kk) for (int dt = 0; dt < 2; ++dt) for (int l = 0; l < 64; ++l) {
    const int d = 64 * ctile + 32 * dt + (l & 31); const int h = l >> 5; const int im = kk & 1; const int kb = 16 * (kk >> 1) + 4 * h;
    for (int e = 0; e < 8; ++e) {
      const int k = kb + 8 * (e >> 2) + (e & 3);
      int idx = (d * k + (im ? 112 : 0)) % 448;
      if (im && k == 0) idx = (d & 1) * 224;
      g.v[((kk * 2 + dt) * 64 + l) * 8 + e] = COS.v[idx];
    }
  }
  return g;
}
struct CT3 { unsigned short v[12288]; };
constexpr CT3 mkC() {
  CT3 c{};
  for (int fr = 0; fr < 8; ++fr) for (int l = 0; l < 64; ++l) for (int e = 0; e < 8; ++e) {
    const int i = 16 * (fr >> 1) + (l & 15), k = 32 * (fr & 1) + 8 * (l >> 4) + e;
    c.v[(fr * 64 + l) * 8 + e] = COS.v[((i * k) & 63) * 7];
    c.v[4096 + (fr * 64 + l) * 8 + e] = COS.v[((i * k + 48) & 63) * 7];
  }
  for (int t = 0; t < 2; ++t) for (int l = 0; l < 64; ++l) for (int e = 0; e < 8; ++e) {
    const int Q = l >> 4; const int i = 16 * t + (l & 15), k = 16 * (Q >> 1) + 8 * (e >> 2) + 4 * (Q & 1) + (e & 3);
    c.v[8192 + (t * 64 + l) * 8 + e] = COS.v[((2 * i * k) & 63) * 7];
    c.v[9216 + (t * 64 + l) * 8 + e] = COS.v[((2 * i * k + 48) & 63) * 7];
  }
  return c;
}
constexpr Part G0 = mkG(0), G1 = mkG(1), G2 = mkG(2), G3 = mkG(3), G4 = mkG(4), G5 = mkG(5), G6 = mkG(6);
constexpr CT3 CC = mkC();
struct Tabs { Part g[7]; CT3 c; };
}
__device__ __attribute__((aligned(256))) const ct::Tabs TAB = {{ct::G0, ct::G1, ct::G2, ct::G3, ct::G4, ct::G5, ct::G6}, ct::CC};

namespace gf {
typedef __bf16 bf16x8 __attribute__((ext_vector_type(8)));
typedef __bf16 bf16x4 __attribute__((ext_vector_type(4)));
typedef __bf16 bf16x2 __attribute__((ext_vector_type(2)));
typedef float f32x4 __attribute__((ext_vector_type(4)));
typedef float f32x16 __attribute__((ext_vector_type(16)));
typedef float f32x2 __attribute__((ext_vector_type(2)));
typedef unsigned u32x4 __attribute__((ext_vector_type(4)));
typedef unsigned u32x2 __attribute__((ext_vector_type(2)));
typedef unsigned long long u64;
#define LAS __attribute__((address_space(3)))
#define DI __device__ __forceinline__

constexpr unsigned TAB_G = 0, TAB_C = 7 * 57344;
constexpr unsigned EP_OFF = 0;
constexpr unsigned F1_OFF = 65536;
constexpr int NREP = 16;
constexpr unsigned FY_OFF = 16384;
constexpr unsigned FW_OFF = 6144;
constexpr unsigned FH_OFF = 81920;
constexpr unsigned X1_OFF = 131072;
constexpr unsigned YT_OFF = X1_OFF + 448 * 4096;
constexpr unsigned WT_OFF = YT_OFF + 448 * 4096;
constexpr unsigned G_OFF  = WT_OFF + 232 * 8192;
constexpr unsigned HF_OFF = G_OFF + 8192;
constexpr unsigned WS_END = HF_OFF + 64u * 56 * 2048;

constexpr int AUX = 15 * 8192;
constexpr int FT = AUX, XB = AUX + 28672;
constexpr int T3_C64 = AUX, T3_C32 = AUX + 16384, T3_X = AUX + 20480, T3_Y = AUX + 30720, T3_Z = T3_Y;
constexpr int LDS_BYTES = 163840;

DI unsigned pk2(float a, float b) { f32x2 v = {a, b}; return __builtin_bit_cast(unsigned, __builtin_convertvector(v, bf16x2)); }
DI float rbf(float a) { return __uint_as_float(pk2(a, 0.f) << 16); }
DI float bflo(unsigned u) { return __uint_as_float(u << 16); }
DI float bfhi(unsigned u) { return __uint_as_float(u & 0xffff0000u); }
DI f32x16 mfma32(u32x4 a, u32x4 b, f32x16 c) { return __builtin_amdgcn_mfma_f32_32x32x16_bf16(__builtin_bit_cast(bf16x8, a), __builtin_bit_cast(bf16x8, b), c, 0, 0, 0); }
DI u32x2 tr_read(unsigned addr) { return __builtin_bit_cast(u32x2, __builtin_amdgcn_ds_read_tr16_b64_v4bf16((LAS bf16x4*)(size_t)addr)); }
DI u32x4 lds_r128(unsigned addr) { return *(LAS u32x4*)(size_t)addr; }
DI void lds_w128(unsigned addr, u32x4 v) { *(LAS u32x4*)(size_t)addr = v; }
DI void lds_w64(unsigned addr, u32x2 v) { *(LAS u32x2*)(size_t)addr = v; }
DI void glds16(const char* sbase, unsigned voff, unsigned ldsdst) {
  asm volatile("s_mov_b32 m0, %2\n\ts_nop 0\n\tglobal_load_lds_dwordx4 %0, %1" :: "v"(voff), "s"(sbase), "s"(ldsdst) : "memory");
}
DI void glds16_sc1(const char* sbase, unsigned voff, unsigned ldsdst) {
  asm volatile("s_mov_b32 m0, %2\n\ts_nop 0\n\tglobal_load_lds_dwordx4 %0, %1 sc1" :: "v"(voff), "s"(sbase), "s"(ldsdst) : "memory");
}
DI void ld128_sc1(u32x4& d, const char* sbase, unsigned voff) { asm volatile("global_load_dwordx4 %0, %1, %2 sc1" : "=v"(d) : "v"(voff), "s"(sbase) : "memory"); }
DI void ld128(u32x4& d, const char* sbase, unsigned voff) { asm volatile("global_load_dwordx4 %0, %1, %2" : "=v"(d) : "v"(voff), "s"(sbase) : "memory"); }
DI void ld64_sc1(u32x2& d, const char* sbase, unsigned voff) { asm volatile("global_load_dwordx2 %0, %1, %2 sc1" : "=v"(d) : "v"(voff), "s"(sbase) : "memory"); }
DI void ld32(unsigned& d, const char* sbase, unsigned voff) { asm volatile("global_load_dword %0, %1, %2" : "=v"(d) : "v"(voff), "s"(sbase) : "memory"); }
DI void ld32_sc1(unsigned& d, const char* sbase, unsigned voff) { asm volatile("global_load_dword %0, %1, %2 sc1" : "=v"(d) : "v"(voff), "s"(sbase) : "memory"); }
#define WAITV(n) asm volatile("s_waitcnt vmcnt(%0)" :: "i"(n) : "memory")
#define TIE(x) asm volatile("" : "+v"(x))
#define WAITL0() asm volatile("s_waitcnt lgkmcnt(0)" ::: "memory")
#define BAR() do { asm volatile("" ::: "memory"); __builtin_amdgcn_s_barrier(); asm volatile("" ::: "memory"); } while (0)
#define LBAR() do { WAITL0(); BAR(); } while (0)
#define SB() __builtin_amdgcn_sched_barrier(0)
DI u32x4 cat(u32x2 a, u32x2 b) { return u32x4{a.x, a.y, b.x, b.y}; }
DI float cosr(float r) { return __builtin_amdgcn_cosf(r); }
DI float sinr(float r) { return __builtin_amdgcn_sinf(r); }
typedef __amdgpu_buffer_rsrc_t rsrc_t;
DI void st128(rsrc_t rs, unsigned off, u32x4 v) { __builtin_amdgcn_raw_buffer_store_b128(v, rs, off, 0, 16); }
DI void st64(rsrc_t rs, unsigned off, u32x2 v) { __builtin_amdgcn_raw_buffer_store_b64(v, rs, off, 0, 16); }
DI void st32(rsrc_t rs, unsigned off, unsigned v) { __builtin_amdgcn_raw_buffer_store_b32(v, rs, off, 0, 16); }
#define SLEEPN 2
constexpr int SPIN_CAP = 1 << 18;
DI void spin_flags(u32x2 f, u32x2 tag, const char* ws, unsigned voff) {
  int spins = 0;
  while (__builtin_amdgcn_ballot_w64(f.x != tag.x || f.y != tag.y) != 0ull) {
    if (++spins > SPIN_CAP) break;
    __builtin_amdgcn_s_sleep(SLEEPN);
    ld64_sc1(f, ws, voff); WAITV(0); TIE(f);
  }
}

DI u32x4 pack8(const f32x16& x, int t) { return u32x4{pk2(x[8 * t], x[8 * t + 1]), pk2(x[8 * t + 2], x[8 * t + 3]), pk2(x[8 * t + 4], x[8 * t + 5]), pk2(x[8 * t + 6], x[8 * t + 7])}; }
DI float opq(float x) { asm("" : "+v"(x)); return x; }
DI f32x4 mfma(u32x4 a, u32x4 b, f32x4 c) { return __builtin_amdgcn_mfma_f32_16x16x32_bf16(__builtin_bit_cast(bf16x8, a), __builtin_bit_cast(bf16x8, b), c, 0, 0, 0); }
DI u32x2 lds_r64(unsigned addr) { return *(LAS u32x2*)(size_t)addr; }
DI u32x4 neg8(u32x4 v) { return v ^ u32x4{0x80008000u, 0x80008000u, 0x80008000u, 0x80008000u}; }
struct T3S {
  u32x4 wv, C0, S0, C1, S1, r0, i0, r1, i1; f32x4 re, im;
  float fc[4], fs[4], ic[4], is[4];
  unsigned rowa, rowb, c32a, c64a, t4, t2, Q;
  DI void init(float sc) {
    int tid = threadIdx.x; TIE(tid);
    const int l = tid & 63, w = tid >> 6, r = l & 15; t4 = w >> 1; t2 = w & 1; Q = l >> 4;
    rowa = (16 * t2 + r) * 144; rowb = 16 * t4 + r; c32a = T3_C32 + t2 * 1024 + l * 16; c64a = T3_C64 + t4 * 2048 + l * 16;
    const float a0 = (float)(16 * t2 + r) * (1.0f / 2048), b0 = (float)(16 * t4 + r) * (1.0f / 2048);
#pragma unroll
    for (int i = 0; i < 4; ++i) {
      float a = (float)(16 * t4 + 4 * Q + i) * a0; fc[i] = cosr(a); fs[i] = sinr(a);
      float b = (float)(16 * t2 + 4 * Q + i) * b0; ic[i] = cosr(b) * sc; is[i] = sinr(b) * sc;
      TIE(fc[i]); TIE(fs[i]); TIE(ic[i]); TIE(is[i]);
    }
  }
  DI void w2(unsigned o, unsigned st, const float (&a)[4], const float (&b)[4]) { lds_w64(o, u32x2{pk2(a[0], a[1]), pk2(a[2], a[3])}); lds_w64(o + st, u32x2{pk2(b[0], b[1]), pk2(b[2], b[3])}); }
  template <int ST> DI void R() {
    if (ST == 0) { C0 = lds_r128(c32a); S0 = lds_r128(c32a + 2048); const unsigned o = T3_Z + (rowb >> 5) * 2048 + Q * 512 + (rowb & 31) * 16; r0 = lds_r128(o); i0 = lds_r128(o + 4096); }
    if (ST == 1 || ST == 2) { const unsigned o = (ST == 1 ? T3_X : T3_Y) + rowa + 16 * Q;
      C0 = lds_r128(c64a); S0 = lds_r128(c64a + 8192); C1 = lds_r128(c64a + 1024); S1 = lds_r128(c64a + 8192 + 1024);
      r0 = lds_r128(o); i0 = lds_r128(o + 32 * 144); r1 = lds_r128(o + 64); i1 = lds_r128(o + 64 + 32 * 144); }
    if (ST == 3) { C0 = lds_r128(c32a); S0 = lds_r128(c32a + 2048); const unsigned o = T3_X + rowb * 80 + (Q >> 1) * 32 + (Q & 1) * 8;
      r0 = cat(lds_r64(o), lds_r64(o + 16)); i0 = cat(lds_r64(o + 64 * 80), lds_r64(o + 64 * 80 + 16)); }
  }
  template <int ST> DI void M() {
    const f32x4 z4 = {0, 0, 0, 0};
    if (ST == 0) { re = mfma(r0, C0, z4); im = mfma(i0, C0, z4); re = mfma(i0, S0, re); im = mfma(neg8(r0), S0, im); }
    if (ST == 1) { re = mfma(C0, r0, z4); im = mfma(C0, i0, z4); re = mfma(S0, i0, re); im = mfma(S0, neg8(r0), im);
                   re = mfma(C1, r1, re); im = mfma(C1, i1, im); re = mfma(S1, i1, re); im = mfma(S1, neg8(r1), im); }
    if (ST == 2) { re = mfma(r0, C0, z4); im = mfma(i0, C0, z4); re = mfma(neg8(i0), S0, re); im = mfma(r0, S0, im);
                   re = mfma(r1, C1, re); im = mfma(i1, C1, im); re = mfma(neg8(i1), S1, re); im = mfma(r1, S1, im); }
    if (ST == 3) { re = mfma(C0, r0, z4); im = mfma(C0, i0, z4); re = mfma(S0, neg8(i0), re); im = mfma(S0, r0, im); }
  }
  template <int ST> DI void V() {
    float a[4], b[4];
#pragma unroll
    for (int i = 0; i < 4; ++i) {
      if (ST == 0) { a[i] = opq(re[i] * fc[i] + opq(im[i] * fs[i])); b[i] = opq(im[i] * fc[i] - opq(re[i] * fs[i])); }
      if (ST == 1) { unsigned q = wv[i]; float wa = bflo(q), wb = bfhi(q); a[i] = opq(re[i] * wa - opq(im[i] * wb)); b[i] = opq(re[i] * wb + opq(im[i] * wa)); }
      if (ST == 2) { a[i] = opq(re[i] * ic[i] - opq(im[i] * is[i])); b[i] = opq(im[i] * ic[i] + opq(re[i] * is[i])); }
      if (ST == 3) { a[i] = re[i]; b[i] = im[i]; }
    }
    if (ST == 0) w2(T3_X + rowa + t4 * 32 + Q * 8, 32 * 144, a, b);
    if (ST == 1) w2(T3_Y + rowa + t4 * 32 + Q * 8, 32 * 144, a, b);
    if (ST == 2) w2(T3_X + rowb * 80 + t2 * 32 + Q * 8, 64 * 80, a, b);
    if (ST == 3) w2(T3_Y + rowb * 64 + t2 * 32 + Q * 8, 4096, a, b);
  }
};

DI void helper(int hw, const unsigned short* x, const unsigned short* wr, const unsigned short* wi, const unsigned short* gwp, const unsigned short* wg, const unsigned short* wu, const unsigned short* wd, char* ws, rsrc_t rs, const unsigned short* outp) {
  const int tid = threadIdx.x, l = tid & 63, w = __builtin_amdgcn_readfirstlane(tid >> 6);

  const unsigned l16 = l * 16;
  const char* tab = (const char*)&TAB;
  u32x2 tag; ld64_sc1(tag, ws, EP_OFF);
  const int bt = hw >> 3, q4 = (hw >> 1) & 3, at = hw & 1;
  const int tr_ = tid / 29, kg = tid - tr_ * 29;
  {
    u32x4 wq[8];
    const int rr = tr_ < 16 ? tr_ : 15;
    const unsigned o = (16 * at + rr + 32 * (16 * bt + 4 * q4)) * 450 + (kg < 28 ? 16 * kg : 434);
#pragma unroll
    for (int i = 0; i < 4; ++i) { ld128(wq[2 * i], (const char*)wr, o + i * 14400); ld128(wq[2 * i + 1], (const char*)wi, o + i * 14400); }
    const int lq = l < 56 ? l : 55;
    u32x4 gv, xv[8];
    ld128(gv, (const char*)gwp, lq * 16);
#pragma unroll
    for (int i = 0; i < 8; ++i) ld128(xv[i], (const char*)x, (64 * hw + 8 * w + i) * 896 + lq * 16);
    unsigned pfv[2];
    { const unsigned gt = hw * 98 + min(l, 48) + (w & 1) * 49; ld32(pfv[0], tab, gt * 128); ld32(pfv[1], tab + TAB_C, min(tid, 159) * 128); }

    WAITV(11); TIE(tag);

#pragma unroll
    for (int i = 0; i < 8; ++i) TIE(wq[i]);
    { u64 t = (((u64)tag.y << 32) | tag.x) + 1; tag = u32x2{(unsigned)t, (unsigned)(t >> 32)}; }
    if (tr_ < 16) {
#pragma unroll
      for (int c = 0; c < 8; ++c) {
        u32x4 v;
#pragma unroll
        for (int i = 0; i < 4; ++i) { unsigned a = wq[2 * i][c >> 1], b = wq[2 * i + 1][c >> 1]; v[i] = __builtin_amdgcn_perm(b, a, (c & 1) ? 0x07060302u : 0x05040100u); }
        lds_w128((8 * kg + c) * 256 + ((tr_ ^ (kg & 15)) << 4), v);
      }
    }
    LBAR();
#pragma unroll
    for (int it = 0; it < 8; ++it) {
      const int ks = min(it * 32 + (tid >> 4), 231), r = tid & 15;
      st128(rs, WT_OFF + ks * 8192 + (bt * 2 + at) * 1024 + (q4 * 16 + r) * 16, lds_r128(ks * 256 + ((r ^ ((ks >> 3) & 15)) << 4)));
    }
    WAITV(10); TIE(gv);
#pragma unroll
    for (int i = 0; i < 8; ++i) TIE(xv[i]);
    float t8[8];
#pragma unroll
    for (int i = 0; i < 8; ++i) {
      float t = 0.f;
#pragma unroll
      for (int e = 0; e < 4; ++e) { unsigned xe = xv[i][e], ge = gv[e]; unsigned pp = pk2(bflo(xe) * bflo(ge), bfhi(xe) * bfhi(ge)); t += bflo(pp) + bfhi(pp); }
      t8[i] = l < 56 ? t : 0.f;
    }
#pragma unroll
    for (int i = 0; i < 4; ++i) { float keep = (l & 1) ? t8[2 * i + 1] : t8[2 * i], send = (l & 1) ? t8[2 * i] : t8[2 * i + 1]; t8[i] = keep + __shfl_xor(send, 1); }
#pragma unroll
    for (int i = 0; i < 2; ++i) { float keep = (l & 2) ? t8[2 * i + 1] : t8[2 * i], send = (l & 2) ? t8[2 * i] : t8[2 * i + 1]; t8[i] = keep + __shfl_xor(send, 2); }
    float gs = ((l & 4) ? t8[1] : t8[0]) + __shfl_xor((l & 4) ? t8[0] : t8[1], 4);
    gs += __shfl_xor(gs, 8); gs += __shfl_xor(gs, 16); gs += __shfl_xor(gs, 32);
    float t = rbf(gs); float g = rbf(1.0f / (1.0f + __expf(-t))); const int s = 64 * hw + 8 * w + l;
    if (l < 8) st32(rs, G_OFF + ((s & 63) * 32 + (s >> 6)) * 4, __float_as_uint(g));
    WAITV(0); TIE(pfv[0]); TIE(pfv[1]);
  }
  WAITV(0);
  BAR();
  if (tid == 0) st64(rs, FW_OFF + hw * 8, tag);
  if (hw < 2) {
#pragma unroll
    for (int i = 0; i < 3; ++i) { const int q = min(i * 8 + w, 19); glds16(tab + TAB_C + q * 1024, l16, T3_C64 + q * 1024); }
    T3S tin; tin.init(1.0f / (2048.0f * 448.0f));
    const unsigned po = l < 32 ? F1_OFF + l * 8 : FW_OFF + (l & 31) * 8;
    if (w == 0) { u32x2 f; ld64_sc1(f, ws, po); WAITV(0); TIE(f); spin_flags(f, tag, ws, po); }
    BAR();
    u32x4 z;
    ld128_sc1(z, ws, X1_OFF + hw * 16 * 4096 + (tid & 255) * 16);
    ld128_sc1(tin.wv, ws, WT_OFF + hw * 231 * 8192 + tid * 16);
    WAITV(0); TIE(z); TIE(tin.wv);
    lds_w128(T3_Z + tid * 16, w < 4 ? z : u32x4{0, 0, 0, 0});
    LBAR();
    tin.R<0>(); tin.M<0>(); tin.V<0>(); LBAR();
    tin.R<1>(); tin.M<1>(); tin.V<1>(); LBAR();
    tin.R<2>(); tin.M<2>(); tin.V<2>(); LBAR();
    tin.R<3>(); tin.M<3>(); tin.V<3>(); LBAR();
    if (w < 4) st128(rs, YT_OFF + hw * 16 * 4096 + tid * 16, lds_r128(T3_Y + tid * 16));
    WAITV(0);
    BAR();
    if (tid < 4 * NREP && (hw || (tid & 3) == 0)) st64(rs, FY_OFF + (tid >> 2) * 2048 + hw * 224 * 8 + (tid & 3) * 8, tag);
  }
}

__global__ __launch_bounds__(512, 2) void fused(const unsigned short* __restrict__ x, const unsigned short* __restrict__ wr, const unsigned short* __restrict__ wi,
                                                const unsigned short* __restrict__ wg, const unsigned short* __restrict__ wu, const unsigned short* __restrict__ wd,
                                                const unsigned short* __restrict__ gwp, char* ws, unsigned short* __restrict__ out) {
  extern __shared__ __attribute__((aligned(16))) char smem[];
  u32x4 cp = {0, 0, 0, 0};
  { const char* pc;
    asm volatile("s_getpc_b64 %1\n\ts_cmp_lt_u32 %3, %4\n\ts_cbranch_scc1 1f\n\tglobal_load_dwordx4 %0, %2, %1\n1:"
                 : "+v"(cp), "=&s"(pc) : "v"(min(((blockIdx.x >> 3 & 3) * 512 + threadIdx.x) * 16, CODEPF - 16u)), "s"(blockIdx.x), "s"(CPFROM) : "memory", "scc"); }
  const rsrc_t rs = __builtin_amdgcn_make_buffer_rsrc(ws, 0, WS_END, 0x00020000);
  const int xcd = blockIdx.x & 7, li = blockIdx.x >> 3;
  if (li >= 28) { helper(xcd * 4 + (li - 28), x, wr, wi, gwp, wg, wu, wd, ws, rs, out); TIE(cp); return; }
  const int lq = li / 7, lr = li - lq * 7;
  const int p = 4 * (xcd >> 1) + lq, j = 7 * (xcd & 1) + lr;
  const int bb = 4 * xcd + lq, ctile = lr;
  const int kcol = xcd * 28 + li, nre = 32 * (kcol >> 4) + (kcol & 15);
  const int tid = threadIdx.x, l = tid & 63;
  const int w = __builtin_amdgcn_readfirstlane(tid >> 6);
  const int m = w & 3, n = w >> 2;
  const bool mhi = (m >> 1) != 0;
  const int a31 = l & 31, h = l >> 5;
  const unsigned ldw = w * 1024;
  const char* tab = (const char*)&TAB;
  const unsigned l16 = l * 16;
  unsigned xoff;
  { int rho = 8 * w + (l >> 3); int mm = rho >> 5, a = rho & 31; int ch = (l & 7) ^ ((rho >> 1) & 7);
    xoff = (64 * a + 4 * p + mm) * 896 + ch * 16; }
  unsigned woff;
  { int rr = 8 * m + (l >> 3); int ct = ((l >> 1) & 3) ^ (((rr >> 1) & 1) | (n << 1));
    woff = rr * 3584 + 256 * j + ct * 32 + (l & 1) * 16; }
  const char* wbase = (const char*)(n ? wu : wg);
  unsigned doff;
  { int c2 = ((l >> 2) & 1) ^ ((l >> 4) & 1);
    doff = (n * 128 + m * 16 + (l >> 3)) * 896 + ctile * 128 + c2 * 64 + (l & 3) * 16; }
  const char* gbase = tab + TAB_G + ctile * 57344;
#define GX(o, dst) glds16((const char*)x + (o), xoff, (dst) + ldw)
#define GW(o, dst) glds16(wbase + (o), woff, (dst) + ldw)
#define GD(o, dst) glds16((const char*)wd + (o), doff, (dst) + ldw)
#define GC(i) do { const int qq = min(8 * (i) + w, 19) * 1024; glds16(tab + TAB_C + qq, l16, T3_C64 + qq); } while (0)
#define GZ() glds16_sc1(ws + X1_OFF + (nre + 16 * n) * 4096 + m * 1024, l16, T3_Z + ldw)
#define GY(o, dst) glds16_sc1(ws + (o), yoff, (dst) + ldw)
  u32x2 tag, pf, pg;
  u32x4 xf[28], ff[8], P0, P1, R0, R1;
  f32x16 accf = {}, ac0 = {}, ac1 = {}, bc0 = {}, bc1 = {};
  unsigned xa[4];
#pragma unroll
  for (int k4 = 0; k4 < 4; ++k4) xa[k4] = ((m & 1) * 32 + a31) * 128 + (((2 * k4 + h) ^ ((a31 >> 1) & 7)) << 4);
  const unsigned fa = FT + l16;
  const int g2 = (l >> 4) & 1, q2 = (l & 15) >> 2;
  unsigned wa0, wa1, wb0, wb1, wc0, wc1;
  { const unsigned wrow = g2 * 4096 + (8 * h + q2) * 128 + (l & 3) * 8; const unsigned sw = (q2 >> 1) | (g2 << 1);
    wa0 = wrow + (((2 * n) ^ sw) << 5); wa1 = wrow + (((2 * n + 1) ^ sw) << 5);
    wb0 = wa0 + 57344; wb1 = wa1 + 57344; wc0 = wa0 + 114688; wc1 = wa1 + 114688; TIE(wb0); TIE(wb1); TIE(wc0); TIE(wc1); }
  const unsigned hst = HF_OFF + (((4 * p + m) * 28 + j) * 4 + 2 * n) * 1024 + l16;
  const unsigned hpo = FH_OFF + (bb >> 1) * 2048 + (bb & 1) * 512 + min(l, 55) * 8;
  const unsigned fho = FH_OFF + p * 2048 + (m >> 1) * 512 + (j * 4 + (m & 1) * 2 + n) * 8;
  const unsigned f1o = F1_OFF + ((j * 4 + m) * 32 + p * 2 + n) * 8;
  const unsigned xpo = l < 32 ? F1_OFF + ((kcol >> 2) * 32 + l) * 8 : FW_OFF + (l & 31) * 8;
  T3S tin;
#define MAKE_TW() tin.init(2.0f / (2048.0f * 448.0f))
#define T3R0() tin.R<0>()
#define T3R1() tin.R<1>()
#define T3R2() tin.R<2>()
#define T3R3() tin.R<3>()
#define T3M0() tin.M<0>()
#define T3M1() tin.M<1>()
#define T3M2() tin.M<2>()
#define T3M3() tin.M<3>()
#define T3V0() tin.V<0>()
#define T3V1() tin.V<1>()
#define T3V2() tin.V<2>()
#define T3V3() tin.V<3>()
#define T3R4() do { if (kcol != 0) st128(rs, YT_OFF + (nre + 16 * n) * 4096 + (tid & 255) * 16, lds_r128(T3_Y + tid * 16)); } while (0)
#define SPINY() do { int spins = 0; u32x4 f4a, f4b; \
    for (;;) { \
      ld128_sc1(f4a, ws, ypo); ld128_sc1(f4b, ws, ypo + 16); WAITV(0); TIE(f4a); TIE(f4b); \
      bool bad = f4a.x != tag.x || f4a.y != tag.y || f4a.z != tag.x || f4a.w != tag.y || f4b.x != tag.x || f4b.y != tag.y || f4b.z != tag.x || f4b.w != tag.y; \
      if (__builtin_amdgcn_ballot_w64(bad) == 0ull || ++spins > SPIN_CAP) break; \
      __builtin_amdgcn_s_sleep(SLEEPN); \
    } \
    if (blockIdx.x == 0 && tid == 0) st64(rs, EP_OFF, tag); } while (0)
  unsigned pfv = 0;
#define SPF() do { const unsigned li_ = kcol * 56 + min(l, 55); \
    const char* pb_ = w == 0 ? (const char*)wd : (const char*)(kcol < 112 ? wg : wu); \
    const unsigned po_ = w == 0 ? li_ * 128 : (li_ % 6272) * 256 + 128; \
    if (w < 2) ld32(pfv, pb_, po_); } while (0)
#define PRESCALE() do { const float og_ = rbf(1.0f - __uint_as_float(gq)), oh_ = rbf(1.0f - __uint_as_float(gq2)); c0 *= og_; c1 *= og_; c2 *= oh_; c3 *= oh_; TIE(c0); TIE(c1); TIE(c2); TIE(c3); } while (0)
  auto FGEN = [&]() {
#pragma unroll 1
    for (int it = tid; it < 28 * 32; it += 512) {
      int kk = it >> 5, hh = (it >> 4) & 1, c = it & 15; int kf = 16 * j + c;
      float cs[8], sn[8];
      float rc = cosr(kf * (1.0f / 448)), rsn = sinr(kf * (1.0f / 448));
      int d0 = 16 * kk + 8 * hh; int idx = (d0 * kf) % 448;
      float c0 = cosr(idx * (1.0f / 448)), s0 = -sinr(idx * (1.0f / 448));
#pragma unroll
      for (int i = 0; i < 8; ++i) { cs[i] = c0; sn[i] = s0; float c1 = c0 * rc + s0 * rsn, s1 = s0 * rc - c0 * rsn; c0 = c1; s0 = s1; }
      u32x4 sv = {pk2(sn[0], sn[1]), pk2(sn[2], sn[3]), pk2(sn[4], sn[5]), pk2(sn[6], sn[7])};
      if (kf == 0) sv = u32x4{0xbf803f80u, 0xbf803f80u, 0xbf803f80u, 0xbf803f80u};
      lds_w128(FT + kk * 1024 + (hh * 32 + c) * 16, u32x4{pk2(cs[0], cs[1]), pk2(cs[2], cs[3]), pk2(cs[4], cs[5]), pk2(cs[6], cs[7])});
      lds_w128(FT + kk * 1024 + (hh * 32 + 16 + c) * 16, sv);
    }
  };
  float hv[16];
  auto gelu1 = [&](float g, float u) {
    float z = g * (-2.302208198f - 0.1029432397f * g * g);
    float sg = __builtin_amdgcn_rcpf(1.0f + __builtin_amdgcn_exp2f(z));
    return g * sg * u;
  };
#define PK8(o) u32x4{pk2(hv[o], hv[o + 1]), pk2(hv[o + 2], hv[o + 3]), pk2(hv[o + 4], hv[o + 5]), pk2(hv[o + 6], hv[o + 7])}
#define TAGINC() do { TIE(pfv); TIE(tag); u64 t_ = (((u64)tag.y << 32) | tag.x) + 1; tag = u32x2{(unsigned)t_, (unsigned)(t_ >> 32)}; } while (0)
#define X1BOUNCE() do { lds_w128(XB + a31 * 256 + (((h * 4 + m) ^ (a31 & 15)) << 4), pack8(accf, 0)); lds_w128(XB + a31 * 256 + (((8 + h * 4 + m) ^ (a31 & 15)) << 4), pack8(accf, 1)); } while (0)
#define X1STORE() st128(rs, X1_OFF + (32 * j + (tid >> 4)) * 4096 + (p >> 3) * 2048 + ((tid >> 2) & 3) * 512 + ((4 * p + (tid & 3)) & 31) * 16, \
                        lds_r128(XB + (tid >> 4) * 256 + (((tid & 15) ^ ((tid >> 4) & 15)) << 4)))
#define P2SETUP() \
    const unsigned ra = ldw + (4 * h + q2) * 128 + g2 * 32 + (l & 3) * 8; \
    const unsigned ra0 = ra + ((q2 >> 1) << 6), ra1 = ra + (((q2 >> 1) ^ 1) << 6); \
    unsigned rb0 = ra0 + 57344, rb1 = ra1 + 57344, rc0 = ra0 + 114688, rc1 = ra1 + 114688; TIE(rb0); TIE(rb1); TIE(rc0); TIE(rc1); \
    const unsigned hb = HF_OFF + (2 * bb * 112 + w) * 1024 + l16; \
    const unsigned gqo = G_OFF + (2 * bb * 32 + a31) * 4; \
    const unsigned ypo = FY_OFF + (blockIdx.x & (NREP - 1)) * 2048 + (l < 56 ? l : 56) * 32; \
    const unsigned yoff = YT_OFF + (16 * w + (l >> 3)) * 4096 + bb * 128 + ((((l >> 2) & 1) ^ ((l >> 4) & 1)) << 6) + (l & 3) * 16; \
    const unsigned gvo = w * 2048 + l16; \
    f32x16 c0 = {}, c1 = {}, c2 = {}, c3 = {}, e0 = {}, e1 = {}, e2 = {}, e3 = {}; \
    u32x4 hf[14], gf[8]; unsigned gq, gq2
  {
    GX(0, 0);
    GX(1792, 8192);
    GW(0, 16384);
    GW(114688, 24576);
    GX(128, 32768);
    GX(1920, 40960);
    GW(229376, 49152);
    GW(344064, 57344);
    ld64_sc1(tag, ws, EP_OFF);
    SPF();
    FGEN();
    MAKE_TW();
    WAITV(5);
    LBAR();
    { const unsigned sb = mhi ? 8192 : 0;
      xf[0] = lds_r128(sb + xa[0]);
      xf[1] = lds_r128(sb + xa[1]);
      xf[2] = lds_r128(sb + xa[2]);
      xf[3] = lds_r128(sb + xa[3]);
    }
    P0 = cat(tr_read(wa0 + 16384), tr_read(wa0 + 16896));
    P1 = cat(tr_read(wa1 + 16384), tr_read(wa1 + 16896));
    SB();
    GX(256, 65536);
    GX(2048, 73728);
    R0 = cat(tr_read(wa0 + 18432), tr_read(wa0 + 18944));
    R1 = cat(tr_read(wa1 + 18432), tr_read(wa1 + 18944));
    if (n == 0) { ff[0] = lds_r128(fa + 0); ff[1] = lds_r128(fa + 1024); ff[2] = lds_r128(fa + 2048); ff[3] = lds_r128(fa + 3072); }
    SB();
    ac0 = mfma32(P0, xf[0], ac0);
    SB();
    ac1 = mfma32(P1, xf[0], ac1);
    SB();
    GW(458752, 81920);
    SB();
    P0 = cat(tr_read(wa0 + 24576), tr_read(wa0 + 25088));
    P1 = cat(tr_read(wa1 + 24576), tr_read(wa1 + 25088));
    SB();
    ac0 = mfma32(R0, xf[1], ac0);
    SB();
    ac1 = mfma32(R1, xf[1], ac1);
    SB();
    GW(573440, 90112);
    SB();
    R0 = cat(tr_read(wa0 + 26624), tr_read(wa0 + 27136));
    R1 = cat(tr_read(wa1 + 26624), tr_read(wa1 + 27136));
    SB();
    if (n == 0) { accf = mfma32(xf[0], ff[0], accf); accf = mfma32(xf[1], ff[1], accf); }
    GX(384, 98304);
    SB();
    ac0 = mfma32(P0, xf[2], ac0);
    SB();
    ac1 = mfma32(P1, xf[2], ac1);
    SB();
    GX(2176, 106496);
    SB();
    if (n == 0) { accf = mfma32(xf[2], ff[2], accf); accf = mfma32(xf[3], ff[3], accf); }
    WAITV(7);
    LBAR();
    { const unsigned sb = mhi ? 40960 : 32768;
      xf[4] = lds_r128(sb + xa[0]);
      xf[5] = lds_r128(sb + xa[1]);
      xf[6] = lds_r128(sb + xa[2]);
      xf[7] = lds_r128(sb + xa[3]);
    }
    P0 = cat(tr_read(wa0 + 49152), tr_read(wa0 + 49664));
    P1 = cat(tr_read(wa1 + 49152), tr_read(wa1 + 49664));
    SB();
    ac0 = mfma32(R0, xf[3], ac0);
    SB();
    ac1 = mfma32(R1, xf[3], ac1);
    SB();
    GW(688128, 114688);
    SB();
    R0 = cat(tr_read(wa0 + 51200), tr_read(wa0 + 51712));
    R1 = cat(tr_read(wa1 + 51200), tr_read(wa1 + 51712));
    if (n == 0) { ff[0] = lds_r128(fa + 4096); ff[1] = lds_r128(fa + 5120); ff[2] = lds_r128(fa + 6144); ff[3] = lds_r128(fa + 7168); }
    SB();
    ac0 = mfma32(P0, xf[4], ac0);
    SB();
    ac1 = mfma32(P1, xf[4], ac1);
    SB();
    GW(802816, 0);
    SB();
    P0 = cat(tr_read(wb0 + 0), tr_read(wb0 + 512));
    P1 = cat(tr_read(wb1 + 0), tr_read(wb1 + 512));
    SB();
    ac0 = mfma32(R0, xf[5], ac0);
    SB();
    ac1 = mfma32(R1, xf[5], ac1);
    SB();
    GX(512, 8192);
    SB();
    R0 = cat(tr_read(wb0 + 2048), tr_read(wb0 + 2560));
    R1 = cat(tr_read(wb1 + 2048), tr_read(wb1 + 2560));
    SB();
    if (n == 0) { accf = mfma32(xf[4], ff[0], accf); accf = mfma32(xf[5], ff[1], accf); }
    GX(2304, 16384);
    SB();
    ac0 = mfma32(P0, xf[6], ac0);
    SB();
    ac1 = mfma32(P1, xf[6], ac1);
    SB();
    GW(917504, 24576);
    SB();
    if (n == 0) { accf = mfma32(xf[6], ff[2], accf); accf = mfma32(xf[7], ff[3], accf); }
    WAITV(7);
    LBAR();
    { const unsigned sb = mhi ? 73728 : 65536;
      xf[8] = lds_r128(sb + xa[0]);
      xf[9] = lds_r128(sb + xa[1]);
      xf[10] = lds_r128(sb + xa[2]);
      xf[11] = lds_r128(sb + xa[3]);
    }
    P0 = cat(tr_read(wb0 + 24576), tr_read(wb0 + 25088));
    P1 = cat(tr_read(wb1 + 24576), tr_read(wb1 + 25088));
    SB();
    ac0 = mfma32(R0, xf[7], ac0);
    SB();
    ac1 = mfma32(R1, xf[7], ac1);
    SB();
    GW(1032192, 32768);
    SB();
    R0 = cat(tr_read(wb0 + 26624), tr_read(wb0 + 27136));
    R1 = cat(tr_read(wb1 + 26624), tr_read(wb1 + 27136));
    if (n == 0) { ff[0] = lds_r128(fa + 8192); ff[1] = lds_r128(fa + 9216); ff[2] = lds_r128(fa + 10240); ff[3] = lds_r128(fa + 11264); }
    SB();
    ac0 = mfma32(P0, xf[8], ac0);
    SB();
    ac1 = mfma32(P1, xf[8], ac1);
    SB();
    GX(640, 40960);
    SB();
    P0 = cat(tr_read(wb0 + 32768), tr_read(wb0 + 33280));
    P1 = cat(tr_read(wb1 + 32768), tr_read(wb1 + 33280));
    SB();
    ac0 = mfma32(R0, xf[9], ac0);
    SB();
    ac1 = mfma32(R1, xf[9], ac1);
    SB();
    GX(2432, 49152);
    SB();
    R0 = cat(tr_read(wb0 + 34816), tr_read(wb0 + 35328));
    R1 = cat(tr_read(wb1 + 34816), tr_read(wb1 + 35328));
    SB();
    if (n == 0) { accf = mfma32(xf[8], ff[0], accf); accf = mfma32(xf[9], ff[1], accf); }
    GW(1146880, 57344);
    SB();
    ac0 = mfma32(P0, xf[10], ac0);
    SB();
    ac1 = mfma32(P1, xf[10], ac1);
    SB();
    SB();
    if (n == 0) { accf = mfma32(xf[10], ff[2], accf); accf = mfma32(xf[11], ff[3], accf); }
    WAITV(7);
    LBAR();
    { const unsigned sb = mhi ? 106496 : 98304;
      xf[12] = lds_r128(sb + xa[0]);
      xf[13] = lds_r128(sb + xa[1]);
      xf[14] = lds_r128(sb + xa[2]);
      xf[15] = lds_r128(sb + xa[3]);
    }
    P0 = cat(tr_read(wc0 + 0), tr_read(wc0 + 512));
    P1 = cat(tr_read(wc1 + 0), tr_read(wc1 + 512));
    SB();
    ac0 = mfma32(R0, xf[11], ac0);
    SB();
    ac1 = mfma32(R1, xf[11], ac1);
    SB();
    GW(1261568, 65536);
    SB();
    R0 = cat(tr_read(wc0 + 2048), tr_read(wc0 + 2560));
    R1 = cat(tr_read(wc1 + 2048), tr_read(wc1 + 2560));
    if (n == 0) { ff[0] = lds_r128(fa + 12288); ff[1] = lds_r128(fa + 13312); ff[2] = lds_r128(fa + 14336); ff[3] = lds_r128(fa + 15360); }
    SB();
    ac0 = mfma32(P0, xf[12], ac0);
    SB();
    ac1 = mfma32(P1, xf[12], ac1);
    SB();
    GX(768, 73728);
    SB();
    P0 = cat(tr_read(wa0 + 0), tr_read(wa0 + 512));
    P1 = cat(tr_read(wa1 + 0), tr_read(wa1 + 512));
    SB();
    ac0 = mfma32(R0, xf[13], ac0);
    SB();
    ac1 = mfma32(R1, xf[13], ac1);
    SB();
    GX(2560, 81920);
    SB();
    R0 = cat(tr_read(wa0 + 2048), tr_read(wa0 + 2560));
    R1 = cat(tr_read(wa1 + 2048), tr_read(wa1 + 2560));
    SB();
    if (n == 0) { accf = mfma32(xf[12], ff[0], accf); accf = mfma32(xf[13], ff[1], accf); }
    GW(1376256, 90112);
    SB();
    ac0 = mfma32(P0, xf[14], ac0);
    SB();
    ac1 = mfma32(P1, xf[14], ac1);
    SB();
    SB();
    if (n == 0) { accf = mfma32(xf[14], ff[2], accf); accf = mfma32(xf[15], ff[3], accf); }
    WAITV(7);
    LBAR();
    { const unsigned sb = mhi ? 16384 : 8192;
      xf[16] = lds_r128(sb + xa[0]);
      xf[17] = lds_r128(sb + xa[1]);
      xf[18] = lds_r128(sb + xa[2]);
      xf[19] = lds_r128(sb + xa[3]);
    }
    P0 = cat(tr_read(wa0 + 24576), tr_read(wa0 + 25088));
    P1 = cat(tr_read(wa1 + 24576), tr_read(wa1 + 25088));
    SB();
    ac0 = mfma32(R0, xf[15], ac0);
    SB();
    ac1 = mfma32(R1, xf[15], ac1);
    SB();
    GW(1490944, 98304);
    SB();
    R0 = cat(tr_read(wa0 + 26624), tr_read(wa0 + 27136));
    R1 = cat(tr_read(wa1 + 26624), tr_read(wa1 + 27136));
    if (n == 0) { ff[0] = lds_r128(fa + 16384); ff[1] = lds_r128(fa + 17408); ff[2] = lds_r128(fa + 18432); ff[3] = lds_r128(fa + 19456); }
    SB();
    ac0 = mfma32(P0, xf[16], ac0);
    SB();
    ac1 = mfma32(P1, xf[16], ac1);
    SB();
    GW(128, 106496);
    SB();
    P0 = cat(tr_read(wa0 + 32768), tr_read(wa0 + 33280));
    P1 = cat(tr_read(wa1 + 32768), tr_read(wa1 + 33280));
    SB();
    ac0 = mfma32(R0, xf[17], ac0);
    SB();
    ac1 = mfma32(R1, xf[17], ac1);
    SB();
    GW(114816, 114688);
    SB();
    R0 = cat(tr_read(wa0 + 34816), tr_read(wa0 + 35328));
    R1 = cat(tr_read(wa1 + 34816), tr_read(wa1 + 35328));
    SB();
    if (n == 0) { accf = mfma32(xf[16], ff[0], accf); accf = mfma32(xf[17], ff[1], accf); }
    GW(229504, 0);
    SB();
    ac0 = mfma32(P0, xf[18], ac0);
    SB();
    ac1 = mfma32(P1, xf[18], ac1);
    SB();
    SB();
    if (n == 0) { accf = mfma32(xf[18], ff[2], accf); accf = mfma32(xf[19], ff[3], accf); }
    WAITV(7);
    LBAR();
    { const unsigned sb = mhi ? 49152 : 40960;
      xf[20] = lds_r128(sb + xa[0]);
      xf[21] = lds_r128(sb + xa[1]);
      xf[22] = lds_r128(sb + xa[2]);
      xf[23] = lds_r128(sb + xa[3]);
    }
    P0 = cat(tr_read(wb0 + 0), tr_read(wb0 + 512));
    P1 = cat(tr_read(wb1 + 0), tr_read(wb1 + 512));
    SB();
    ac0 = mfma32(R0, xf[19], ac0);
    SB();
    ac1 = mfma32(R1, xf[19], ac1);
    SB();
    GW(344192, 8192);
    SB();
    R0 = cat(tr_read(wb0 + 2048), tr_read(wb0 + 2560));
    R1 = cat(tr_read(wb1 + 2048), tr_read(wb1 + 2560));
    if (n == 0) { ff[0] = lds_r128(fa + 20480); ff[1] = lds_r128(fa + 21504); ff[2] = lds_r128(fa + 22528); ff[3] = lds_r128(fa + 23552); }
    SB();
    ac0 = mfma32(P0, xf[20], ac0);
    SB();
    ac1 = mfma32(P1, xf[20], ac1);
    SB();
    GW(458880, 16384);
    SB();
    P0 = cat(tr_read(wb0 + 8192), tr_read(wb0 + 8704));
    P1 = cat(tr_read(wb1 + 8192), tr_read(wb1 + 8704));
    SB();
    ac0 = mfma32(R0, xf[21], ac0);
    SB();
    ac1 = mfma32(R1, xf[21], ac1);
    SB();
    GW(573568, 24576);
    SB();
    R0 = cat(tr_read(wb0 + 10240), tr_read(wb0 + 10752));
    R1 = cat(tr_read(wb1 + 10240), tr_read(wb1 + 10752));
    SB();
    if (n == 0) { accf = mfma32(xf[20], ff[0], accf); accf = mfma32(xf[21], ff[1], accf); }
    GW(688256, 32768);
    SB();
    ac0 = mfma32(P0, xf[22], ac0);
    SB();
    ac1 = mfma32(P1, xf[22], ac1);
    SB();
    SB();
    if (n == 0) { accf = mfma32(xf[22], ff[2], accf); accf = mfma32(xf[23], ff[3], accf); }
    WAITV(7);
    LBAR();
    { const unsigned sb = mhi ? 81920 : 73728;
      xf[24] = lds_r128(sb + xa[0]);
      xf[25] = lds_r128(sb + xa[1]);
      xf[26] = lds_r128(sb + xa[2]);
      xf[27] = lds_r128(sb + xa[3]);
    }
    P0 = cat(tr_read(wb0 + 32768), tr_read(wb0 + 33280));
    P1 = cat(tr_read(wb1 + 32768), tr_read(wb1 + 33280));
    SB();
    ac0 = mfma32(R0, xf[23], ac0);
    SB();
    ac1 = mfma32(R1, xf[23], ac1);
    SB();
    GW(802944, 40960);
    SB();
    R0 = cat(tr_read(wb0 + 34816), tr_read(wb0 + 35328));
    R1 = cat(tr_read(wb1 + 34816), tr_read(wb1 + 35328));
    if (n == 0) { ff[0] = lds_r128(fa + 24576); ff[1] = lds_r128(fa + 25600); ff[2] = lds_r128(fa + 26624); ff[3] = lds_r128(fa + 27648); }
    SB();
    ac0 = mfma32(P0, xf[24], ac0);
    SB();
    ac1 = mfma32(P1, xf[24], ac1);
    SB();
    GW(917632, 49152);
    SB();
    P0 = cat(tr_read(wb0 + 40960), tr_read(wb0 + 41472));
    P1 = cat(tr_read(wb1 + 40960), tr_read(wb1 + 41472));
    SB();
    ac0 = mfma32(R0, xf[25], ac0);
    SB();
    ac1 = mfma32(R1, xf[25], ac1);
    SB();
    GW(1032320, 57344);
    SB();
    R0 = cat(tr_read(wb0 + 43008), tr_read(wb0 + 43520));
    R1 = cat(tr_read(wb1 + 43008), tr_read(wb1 + 43520));
    SB();
    if (n == 0) { accf = mfma32(xf[24], ff[0], accf); accf = mfma32(xf[25], ff[1], accf); }
    GW(1147008, 65536);
    SB();
    ac0 = mfma32(P0, xf[26], ac0);
    SB();
    ac1 = mfma32(P1, xf[26], ac1);
    SB();
    SB();
    if (n == 0) { accf = mfma32(xf[26], ff[2], accf); accf = mfma32(xf[27], ff[3], accf); X1BOUNCE(); }
    P2SETUP();
    WAITV(7);
    TAGINC();
    LBAR();
    X1STORE();
    P0 = cat(tr_read(wb0 + 49152), tr_read(wb0 + 49664));
    P1 = cat(tr_read(wb1 + 49152), tr_read(wb1 + 49664));
    SB();
    ac0 = mfma32(R0, xf[27], ac0);
    SB();
    ac1 = mfma32(R1, xf[27], ac1);
    SB();
    GW(1261696, 73728);
    SB();
    R0 = cat(tr_read(wb0 + 51200), tr_read(wb0 + 51712));
    R1 = cat(tr_read(wb1 + 51200), tr_read(wb1 + 51712));
    SB();
    bc0 = mfma32(P0, xf[0], bc0);
    hv[0] = gelu1(ac0[0], ac0[8]); TIE(hv[0]);
    hv[1] = gelu1(ac0[1], ac0[9]); TIE(hv[1]);
    SB();
    bc1 = mfma32(P1, xf[0], bc1);
    hv[2] = gelu1(ac0[2], ac0[10]); TIE(hv[2]);
    hv[3] = gelu1(ac0[3], ac0[11]); TIE(hv[3]);
    SB();
    GW(1376384, 81920);
    SB();
    P0 = cat(tr_read(wc0 + 0), tr_read(wc0 + 512));
    P1 = cat(tr_read(wc1 + 0), tr_read(wc1 + 512));
    SB();
    bc0 = mfma32(R0, xf[1], bc0);
    hv[4] = gelu1(ac0[4], ac0[12]); TIE(hv[4]);
    hv[5] = gelu1(ac0[5], ac0[13]); TIE(hv[5]);
    SB();
    bc1 = mfma32(R1, xf[1], bc1);
    hv[6] = gelu1(ac0[6], ac0[14]); TIE(hv[6]);
    hv[7] = gelu1(ac0[7], ac0[15]); TIE(hv[7]);
    SB();
    GW(1491072, 90112);
    SB();
    R0 = cat(tr_read(wc0 + 2048), tr_read(wc0 + 2560));
    R1 = cat(tr_read(wc1 + 2048), tr_read(wc1 + 2560));
    SB();
    bc0 = mfma32(P0, xf[2], bc0);
    hv[8] = gelu1(ac1[0], ac1[8]); TIE(hv[8]);
    hv[9] = gelu1(ac1[1], ac1[9]); TIE(hv[9]);
    SB();
    bc1 = mfma32(P1, xf[2], bc1);
    hv[10] = gelu1(ac1[2], ac1[10]); TIE(hv[10]);
    hv[11] = gelu1(ac1[3], ac1[11]); TIE(hv[11]);
    SB();
    SB();
    P0 = cat(tr_read(wa0 + 0), tr_read(wa0 + 512));
    P1 = cat(tr_read(wa1 + 0), tr_read(wa1 + 512));
    SB();
    bc0 = mfma32(R0, xf[3], bc0);
    hv[12] = gelu1(ac1[4], ac1[12]); TIE(hv[12]);
    hv[13] = gelu1(ac1[5], ac1[13]); TIE(hv[13]);
    SB();
    bc1 = mfma32(R1, xf[3], bc1);
    hv[14] = gelu1(ac1[6], ac1[14]); TIE(hv[14]);
    hv[15] = gelu1(ac1[7], ac1[15]); TIE(hv[15]);
    SB();
    SB();
    R0 = cat(tr_read(wa0 + 2048), tr_read(wa0 + 2560));
    R1 = cat(tr_read(wa1 + 2048), tr_read(wa1 + 2560));
    SB();
    bc0 = mfma32(P0, xf[4], bc0);
    SB();
    bc1 = mfma32(P1, xf[4], bc1);
    SB();
    SB();
    st128(rs, hst, PK8(0));
    st128(rs, hst + 1024, PK8(8));
    WAITV(5);
    if (l == 0) st64(rs, f1o, tag);
    P0 = cat(tr_read(wa0 + 8192), tr_read(wa0 + 8704));
    P1 = cat(tr_read(wa1 + 8192), tr_read(wa1 + 8704));
    SB();
    bc0 = mfma32(R0, xf[5], bc0);
    SB();
    bc1 = mfma32(R1, xf[5], bc1);
    SB();
    SB();
    R0 = cat(tr_read(wa0 + 10240), tr_read(wa0 + 10752));
    R1 = cat(tr_read(wa1 + 10240), tr_read(wa1 + 10752));
    SB();
    bc0 = mfma32(P0, xf[6], bc0);
    SB();
    bc1 = mfma32(P1, xf[6], bc1);
    SB();
    SB();
    WAITV(9);
    LBAR();
    P0 = cat(tr_read(wa0 + 16384), tr_read(wa0 + 16896));
    P1 = cat(tr_read(wa1 + 16384), tr_read(wa1 + 16896));
    SB();
    bc0 = mfma32(R0, xf[7], bc0);
    SB();
    bc1 = mfma32(R1, xf[7], bc1);
    SB();
    GC(0);
    SB();
    R0 = cat(tr_read(wa0 + 18432), tr_read(wa0 + 18944));
    R1 = cat(tr_read(wa1 + 18432), tr_read(wa1 + 18944));
    SB();
    bc0 = mfma32(P0, xf[8], bc0);
    SB();
    bc1 = mfma32(P1, xf[8], bc1);
    SB();
    GC(1);
    SB();
    P0 = cat(tr_read(wa0 + 24576), tr_read(wa0 + 25088));
    P1 = cat(tr_read(wa1 + 24576), tr_read(wa1 + 25088));
    SB();
    bc0 = mfma32(R0, xf[9], bc0);
    SB();
    bc1 = mfma32(R1, xf[9], bc1);
    SB();
    GC(2);
    SB();
    R0 = cat(tr_read(wa0 + 26624), tr_read(wa0 + 27136));
    R1 = cat(tr_read(wa1 + 26624), tr_read(wa1 + 27136));
    SB();
    bc0 = mfma32(P0, xf[10], bc0);
    SB();
    bc1 = mfma32(P1, xf[10], bc1);
    SB();
    GD(0, 98304);
    SB();
    P0 = cat(tr_read(wa0 + 32768), tr_read(wa0 + 33280));
    P1 = cat(tr_read(wa1 + 32768), tr_read(wa1 + 33280));
    SB();
    bc0 = mfma32(R0, xf[11], bc0);
    SB();
    bc1 = mfma32(R1, xf[11], bc1);
    SB();
    GD(7168, 106496);
    SB();
    R0 = cat(tr_read(wa0 + 34816), tr_read(wa0 + 35328));
    R1 = cat(tr_read(wa1 + 34816), tr_read(wa1 + 35328));
    SB();
    bc0 = mfma32(P0, xf[12], bc0);
    SB();
    bc1 = mfma32(P1, xf[12], bc1);
    SB();
    GD(229376, 114688);
    SB();
    WAITV(6);
    if (l == 0) st64(rs, fho, tag);
    P0 = cat(tr_read(wa0 + 40960), tr_read(wa0 + 41472));
    P1 = cat(tr_read(wa1 + 40960), tr_read(wa1 + 41472));
    SB();
    bc0 = mfma32(R0, xf[13], bc0);
    SB();
    bc1 = mfma32(R1, xf[13], bc1);
    SB();
    GD(236544, 0);
    SB();
    R0 = cat(tr_read(wa0 + 43008), tr_read(wa0 + 43520));
    R1 = cat(tr_read(wa1 + 43008), tr_read(wa1 + 43520));
    SB();
    bc0 = mfma32(P0, xf[14], bc0);
    SB();
    bc1 = mfma32(P1, xf[14], bc1);
    SB();
    GD(458752, 8192);
    SB();
    WAITV(12);
    LBAR();
    P0 = cat(tr_read(wa0 + 49152), tr_read(wa0 + 49664));
    P1 = cat(tr_read(wa1 + 49152), tr_read(wa1 + 49664));
    SB();
    bc0 = mfma32(R0, xf[15], bc0);
    SB();
    bc1 = mfma32(R1, xf[15], bc1);
    SB();
    GD(465920, 16384);
    SB();
    R0 = cat(tr_read(wa0 + 51200), tr_read(wa0 + 51712));
    R1 = cat(tr_read(wa1 + 51200), tr_read(wa1 + 51712));
    SB();
    bc0 = mfma32(P0, xf[16], bc0);
    SB();
    bc1 = mfma32(P1, xf[16], bc1);
    SB();
    GD(688128, 24576);
    SB();
    P0 = cat(tr_read(wb0 + 0), tr_read(wb0 + 512));
    P1 = cat(tr_read(wb1 + 0), tr_read(wb1 + 512));
    SB();
    bc0 = mfma32(R0, xf[17], bc0);
    SB();
    bc1 = mfma32(R1, xf[17], bc1);
    SB();
    GD(695296, 32768);
    SB();
    R0 = cat(tr_read(wb0 + 2048), tr_read(wb0 + 2560));
    R1 = cat(tr_read(wb1 + 2048), tr_read(wb1 + 2560));
    SB();
    bc0 = mfma32(P0, xf[18], bc0);
    SB();
    bc1 = mfma32(P1, xf[18], bc1);
    SB();
    GD(917504, 40960);
    SB();
    P0 = cat(tr_read(wb0 + 8192), tr_read(wb0 + 8704));
    P1 = cat(tr_read(wb1 + 8192), tr_read(wb1 + 8704));
    SB();
    bc0 = mfma32(R0, xf[19], bc0);
    SB();
    bc1 = mfma32(R1, xf[19], bc1);
    SB();
    SB();
    R0 = cat(tr_read(wb0 + 10240), tr_read(wb0 + 10752));
    R1 = cat(tr_read(wb1 + 10240), tr_read(wb1 + 10752));
    SB();
    bc0 = mfma32(P0, xf[20], bc0);
    SB();
    bc1 = mfma32(P1, xf[20], bc1);
    SB();
    SB();
    P0 = cat(tr_read(wb0 + 16384), tr_read(wb0 + 16896));
    P1 = cat(tr_read(wb1 + 16384), tr_read(wb1 + 16896));
    SB();
    bc0 = mfma32(R0, xf[21], bc0);
    SB();
    bc1 = mfma32(R1, xf[21], bc1);
    SB();
    SB();
    R0 = cat(tr_read(wb0 + 18432), tr_read(wb0 + 18944));
    R1 = cat(tr_read(wb1 + 18432), tr_read(wb1 + 18944));
    SB();
    bc0 = mfma32(P0, xf[22], bc0);
    SB();
    bc1 = mfma32(P1, xf[22], bc1);
    SB();
    SB();
    if (w == 0) ld64_sc1(pg, ws, xpo);
    if (w == 0) ld64_sc1(pf, ws, hpo);
    if (w == 0) { WAITV(0); TIE(pg); spin_flags(pg, tag, ws, xpo); }
    if (w == 0) { WAITV(0); TIE(pf); spin_flags(pf, tag, ws, hpo); }
    WAITV(9);
    LBAR();
    GZ();
    ld128_sc1(tin.wv, ws, WT_OFF + kcol * 8192 + tid * 16);
    P0 = cat(tr_read(wb0 + 24576), tr_read(wb0 + 25088));
    P1 = cat(tr_read(wb1 + 24576), tr_read(wb1 + 25088));
    SB();
    bc0 = mfma32(R0, xf[23], bc0);
    SB();
    bc1 = mfma32(R1, xf[23], bc1);
    SB();
    ld128_sc1(hf[0], ws, hb + 0);
    SB();
    R0 = cat(tr_read(wb0 + 26624), tr_read(wb0 + 27136));
    R1 = cat(tr_read(wb1 + 26624), tr_read(wb1 + 27136));
    SB();
    bc0 = mfma32(P0, xf[24], bc0);
    SB();
    bc1 = mfma32(P1, xf[24], bc1);
    SB();
    ld128_sc1(hf[1], ws, hb + 114688);
    SB();
    P0 = cat(tr_read(wb0 + 32768), tr_read(wb0 + 33280));
    P1 = cat(tr_read(wb1 + 32768), tr_read(wb1 + 33280));
    SB();
    bc0 = mfma32(R0, xf[25], bc0);
    SB();
    bc1 = mfma32(R1, xf[25], bc1);
    SB();
    ld128_sc1(hf[2], ws, hb + 8192);
    SB();
    R0 = cat(tr_read(wb0 + 34816), tr_read(wb0 + 35328));
    R1 = cat(tr_read(wb1 + 34816), tr_read(wb1 + 35328));
    SB();
    bc0 = mfma32(P0, xf[26], bc0);
    SB();
    bc1 = mfma32(P1, xf[26], bc1);
    SB();
    ld128_sc1(hf[3], ws, hb + 122880);
    SB();
    bc0 = mfma32(R0, xf[27], bc0);
    SB();
    bc1 = mfma32(R1, xf[27], bc1);
    SB();
    ld128_sc1(hf[4], ws, hb + 16384);
    SB();
    hv[0] = gelu1(bc0[0], bc0[8]); TIE(hv[0]);
    hv[1] = gelu1(bc0[1], bc0[9]); TIE(hv[1]);
    ld128_sc1(hf[5], ws, hb + 131072);
    hv[2] = gelu1(bc0[2], bc0[10]); TIE(hv[2]);
    hv[3] = gelu1(bc0[3], bc0[11]); TIE(hv[3]);
    ld128_sc1(hf[6], ws, hb + 24576);
    hv[4] = gelu1(bc0[4], bc0[12]); TIE(hv[4]);
    hv[5] = gelu1(bc0[5], bc0[13]); TIE(hv[5]);
    ld128_sc1(hf[7], ws, hb + 139264);
    hv[6] = gelu1(bc0[6], bc0[14]); TIE(hv[6]);
    hv[7] = gelu1(bc0[7], bc0[15]); TIE(hv[7]);
    ld128_sc1(hf[8], ws, hb + 32768);
    st128(rs, hst + 57344, PK8(0));
    hv[8] = gelu1(bc1[0], bc1[8]); TIE(hv[8]);
    hv[9] = gelu1(bc1[1], bc1[9]); TIE(hv[9]);
    ld128_sc1(hf[9], ws, hb + 147456);
    hv[10] = gelu1(bc1[2], bc1[10]); TIE(hv[10]);
    hv[11] = gelu1(bc1[3], bc1[11]); TIE(hv[11]);
    ld128_sc1(hf[10], ws, hb + 40960);
    hv[12] = gelu1(bc1[4], bc1[12]); TIE(hv[12]);
    hv[13] = gelu1(bc1[5], bc1[13]); TIE(hv[13]);
    ld128_sc1(hf[11], ws, hb + 155648);
    hv[14] = gelu1(bc1[6], bc1[14]); TIE(hv[14]);
    hv[15] = gelu1(bc1[7], bc1[15]); TIE(hv[15]);
    ld128_sc1(hf[12], ws, hb + 49152);
    st128(rs, hst + 57344 + 1024, PK8(8));
    WAITV(15);
    TIE(tin.wv);
    LBAR();
    T3R0();
    SB();
    ld128_sc1(hf[13], ws, hb + 163840);
    ld128(gf[0], gbase, gvo + 0);
    T3M0();
    SB();
    ld128(gf[1], gbase, gvo + 1024);
    WAITV(16);
    TIE(hf[0]); TIE(hf[1]);
    P0 = cat(tr_read(rb0 + 40960), tr_read(rb0 + 49152));
    P1 = cat(tr_read(rb1 + 40960), tr_read(rb1 + 49152));
    SB();
    GD(924672, 49152);
    SB();
    c0 = mfma32(P0, hf[0], c0); c1 = mfma32(P1, hf[0], c1);
    SB();
    GD(1146880, 57344);
    SB();
    c2 = mfma32(P0, hf[1], c2); c3 = mfma32(P1, hf[1], c3);
    SB();
    GD(1154048, 65536);
    SB();
    T3V0();
    SB();
    LBAR();
    T3R1();
    SB();
    GD(1376256, 73728);
    ld128(gf[2], gbase, gvo + 16384);
    T3M1();
    SB();
    ld128(gf[3], gbase, gvo + 17408);
    WAITV(20);
    TIE(hf[2]); TIE(hf[3]);
    R0 = cat(tr_read(rc0 + 0), tr_read(ra0 + 0));
    R1 = cat(tr_read(rc1 + 0), tr_read(ra1 + 0));
    SB();
    GD(1383424, 81920);
    SB();
    c0 = mfma32(R0, hf[2], c0); c1 = mfma32(R1, hf[2], c1);
    SB();
    GD(57344, 90112);
    SB();
    c2 = mfma32(R0, hf[3], c2); c3 = mfma32(R1, hf[3], c3);
    SB();
    GD(64512, 98304);
    SB();
    T3V1();
    SB();
    WAITV(12);
    if (l == 0) st64(rs, fho + 1024, tag);
    LBAR();
    T3R2();
    SB();
    GD(286720, 106496);
    ld128(gf[4], gbase, gvo + 32768);
    T3M2();
    SB();
    ld128(gf[5], gbase, gvo + 33792);
    WAITV(24);
    TIE(hf[4]); TIE(hf[5]);
    P0 = cat(tr_read(ra0 + 8192), tr_read(ra0 + 16384));
    P1 = cat(tr_read(ra1 + 8192), tr_read(ra1 + 16384));
    SB();
    GD(293888, 114688);
    SB();
    c0 = mfma32(P0, hf[4], c0); c1 = mfma32(P1, hf[4], c1);
    SB();
    GD(516096, 0);
    SB();
    c2 = mfma32(P0, hf[5], c2); c3 = mfma32(P1, hf[5], c3);
    SB();
    GD(523264, 8192);
    SB();
    T3V2();
    SB();
    LBAR();
    T3R3();
    SB();
    GD(745472, 16384);
    ld128(gf[6], gbase, gvo + 49152);
    T3M3();
    SB();
    ld128(gf[7], gbase, gvo + 50176);
    WAITV(28);
    TIE(hf[6]); TIE(hf[7]);
    R0 = cat(tr_read(ra0 + 24576), tr_read(ra0 + 32768));
    R1 = cat(tr_read(ra1 + 24576), tr_read(ra1 + 32768));
    SB();
    SB();
    c0 = mfma32(R0, hf[6], c0); c1 = mfma32(R1, hf[6], c1);
    SB();
    GD(752640, 24576);
    SB();
    c2 = mfma32(R0, hf[7], c2); c3 = mfma32(R1, hf[7], c3);
    SB();
    GD(974848, 32768);
    SB();
    T3V3();
    SB();
    LBAR();
    T3R4();
    ld32_sc1(gq, ws, gqo);
    ld32_sc1(gq2, ws, gqo + 128);
    if (w == 0) ld64_sc1(pf, ws, hpo + 1024);
    WAITV(21);
    TIE(hf[8]); TIE(hf[9]);
    P0 = cat(tr_read(ra0 + 40960), tr_read(ra0 + 49152));
    P1 = cat(tr_read(ra1 + 40960), tr_read(ra1 + 49152));
    SB();
    SB();
    c0 = mfma32(P0, hf[8], c0); c1 = mfma32(P1, hf[8], c1);
    SB();
    GD(982016, 40960);
    SB();
    c2 = mfma32(P0, hf[9], c2); c3 = mfma32(P1, hf[9], c3);
    SB();
    GD(1204224, 49152);
    SB();
    WAITV(21);
    TIE(hf[10]); TIE(hf[11]);
    R0 = cat(tr_read(rb0 + 0), tr_read(rb0 + 8192));
    R1 = cat(tr_read(rb1 + 0), tr_read(rb1 + 8192));
    SB();
    SB();
    c0 = mfma32(R0, hf[10], c0); c1 = mfma32(R1, hf[10], c1);
    SB();
    GD(1211392, 57344);
    SB();
    c2 = mfma32(R0, hf[11], c2); c3 = mfma32(R1, hf[11], c3);
    SB();
    GD(1433600, 65536);
    SB();
    WAITV(19);
    TIE(hf[12]); TIE(hf[13]);
    P0 = cat(tr_read(rb0 + 16384), tr_read(rb0 + 24576));
    P1 = cat(tr_read(rb1 + 16384), tr_read(rb1 + 24576));
    SB();
    SB();
    c0 = mfma32(P0, hf[12], c0); c1 = mfma32(P1, hf[12], c1);
    SB();
    GD(1440768, 73728);
    SB();
    c2 = mfma32(P0, hf[13], c2); c3 = mfma32(P1, hf[13], c3);
    SB();
    SB();
    if (w == 0) { WAITV(5); TIE(pf); spin_flags(pf, tag, ws, hpo + 1024); }
    WAITV(5);
    TIE(gq); TIE(gq2);
    BAR();
    if (tid < NREP && kcol != 0) st64(rs, FY_OFF + tid * 2048 + kcol * 8, tag);
    ld128_sc1(hf[0], ws, hb + 57344);
    ld128_sc1(hf[1], ws, hb + 172032);
    ld128_sc1(hf[2], ws, hb + 65536);
    ld128_sc1(hf[3], ws, hb + 180224);
    ld128_sc1(hf[4], ws, hb + 73728);
    ld128_sc1(hf[5], ws, hb + 188416);
    WAITV(4);
    TIE(hf[0]); TIE(hf[1]);
    R0 = cat(tr_read(rb0 + 32768), tr_read(rb0 + 40960));
    R1 = cat(tr_read(rb1 + 32768), tr_read(rb1 + 40960));
    SB();
    ld128_sc1(hf[6], ws, hb + 81920);
    SB();
    c0 = mfma32(R0, hf[0], c0); c1 = mfma32(R1, hf[0], c1);
    SB();
    ld128_sc1(hf[7], ws, hb + 196608);
    SB();
    c2 = mfma32(R0, hf[1], c2); c3 = mfma32(R1, hf[1], c3);
    SB();
    ld128_sc1(hf[8], ws, hb + 90112);
    SB();
    WAITV(5);
    TIE(hf[2]); TIE(hf[3]);
    P0 = cat(tr_read(rb0 + 49152), tr_read(rc0 + 0));
    P1 = cat(tr_read(rb1 + 49152), tr_read(rc1 + 0));
    SB();
    ld128_sc1(hf[9], ws, hb + 204800);
    SB();
    c0 = mfma32(P0, hf[2], c0); c1 = mfma32(P1, hf[2], c1);
    SB();
    ld128_sc1(hf[10], ws, hb + 98304);
    SB();
    c2 = mfma32(P0, hf[3], c2); c3 = mfma32(P1, hf[3], c3);
    SB();
    ld128_sc1(hf[11], ws, hb + 212992);
    SB();
    WAITV(6);
    TIE(hf[4]); TIE(hf[5]);
    R0 = cat(tr_read(ra0 + 0), tr_read(ra0 + 8192));
    R1 = cat(tr_read(ra1 + 0), tr_read(ra1 + 8192));
    SB();
    ld128_sc1(hf[12], ws, hb + 106496);
    SB();
    c0 = mfma32(R0, hf[4], c0); c1 = mfma32(R1, hf[4], c1);
    SB();
    ld128_sc1(hf[13], ws, hb + 221184);
    SB();
    c2 = mfma32(R0, hf[5], c2); c3 = mfma32(R1, hf[5], c3);
    SB();
    SB();
    WAITV(6);
    TIE(hf[6]); TIE(hf[7]);
    P0 = cat(tr_read(ra0 + 16384), tr_read(ra0 + 24576));
    P1 = cat(tr_read(ra1 + 16384), tr_read(ra1 + 24576));
    SB();
    SB();
    c0 = mfma32(P0, hf[6], c0); c1 = mfma32(P1, hf[6], c1);
    SB();
    SB();
    c2 = mfma32(P0, hf[7], c2); c3 = mfma32(P1, hf[7], c3);
    SB();
    SB();
    WAITV(4);
    TIE(hf[8]); TIE(hf[9]);
    R0 = cat(tr_read(ra0 + 32768), tr_read(ra0 + 40960));
    R1 = cat(tr_read(ra1 + 32768), tr_read(ra1 + 40960));
    SB();
    SB();
    c0 = mfma32(R0, hf[8], c0); c1 = mfma32(R1, hf[8], c1);
    SB();
    SB();
    c2 = mfma32(R0, hf[9], c2); c3 = mfma32(R1, hf[9], c3);
    SB();
    SB();
    if (w == 0) SPINY();
    BAR();
    GY(0, 81920);
    GY(32768, 122880);
    GY(524288, 131072);
    GY(557056, 139264);
    GY(1048576, 147456);
    GY(1081344, 155648);
    GY(1572864, 90112);
    GY(1605632, 98304);
    WAITV(10);
    TIE(hf[10]); TIE(hf[11]);
    P0 = cat(tr_read(ra0 + 49152), tr_read(rb0 + 0));
    P1 = cat(tr_read(ra1 + 49152), tr_read(rb1 + 0));
    SB();
    SB();
    c0 = mfma32(P0, hf[10], c0); c1 = mfma32(P1, hf[10], c1);
    SB();
    SB();
    c2 = mfma32(P0, hf[11], c2); c3 = mfma32(P1, hf[11], c3);
    SB();
    SB();
    WAITV(8);
    TIE(hf[12]); TIE(hf[13]);
    R0 = cat(tr_read(rb0 + 8192), tr_read(rb0 + 16384));
    R1 = cat(tr_read(rb1 + 8192), tr_read(rb1 + 16384));
    SB();
    SB();
    c0 = mfma32(R0, hf[12], c0); c1 = mfma32(R1, hf[12], c1);
    SB();
    SB();
    c2 = mfma32(R0, hf[13], c2); c3 = mfma32(R1, hf[13], c3);
    SB();
    SB();
    PRESCALE();
    WAITV(6);
    TIE(gf[0]); TIE(gf[1]); TIE(gf[2]); TIE(gf[3]); TIE(gf[4]); TIE(gf[5]); TIE(gf[6]); TIE(gf[7]);
    P0 = cat(tr_read(rb0 + 24576), tr_read(rc0 + 8192));
    P1 = cat(tr_read(rb1 + 24576), tr_read(rc1 + 8192));
    e0 = mfma32(gf[0], P0, e0); e1 = mfma32(gf[1], P0, e1); e2 = mfma32(gf[0], P1, e2); e3 = mfma32(gf[1], P1, e3);
    WAITV(4);
    R0 = cat(tr_read(rc0 + 16384), tr_read(rc0 + 24576));
    R1 = cat(tr_read(rc1 + 16384), tr_read(rc1 + 24576));
    e0 = mfma32(gf[2], R0, e0); e1 = mfma32(gf[3], R0, e1); e2 = mfma32(gf[2], R1, e2); e3 = mfma32(gf[3], R1, e3);
    WAITV(2);
    P0 = cat(tr_read(rc0 + 32768), tr_read(rc0 + 40960));
    P1 = cat(tr_read(rc1 + 32768), tr_read(rc1 + 40960));
    e0 = mfma32(gf[4], P0, e0); e1 = mfma32(gf[5], P0, e1); e2 = mfma32(gf[4], P1, e2); e3 = mfma32(gf[5], P1, e3);
    WAITV(0);
    if (w < 4) {
    R0 = cat(tr_read(rb0 + 32768), tr_read(rb0 + 40960));
    R1 = cat(tr_read(rb1 + 32768), tr_read(rb1 + 40960));
    e0 = mfma32(gf[6], R0, e0); e1 = mfma32(gf[7], R0, e1); e2 = mfma32(gf[6], R1, e2); e3 = mfma32(gf[7], R1, e3);
    }

    LBAR();
    {
      const float g = __uint_as_float(gq), g2_ = __uint_as_float(gq2);
      const unsigned wbb = w * 17408 + a31 * 272 + h * 16;
#pragma unroll
      for (int q = 0; q < 4; ++q) {
        f32x4 v0, v1, v2, v3;
#pragma unroll
        for (int e = 0; e < 4; ++e) { v0[e] = e0[4 * q + e] * g + c0[4 * q + e]; v1[e] = e1[4 * q + e] * g + c1[4 * q + e]; v2[e] = e2[4 * q + e] * g2_ + c2[4 * q + e]; v3[e] = e3[4 * q + e] * g2_ + c3[4 * q + e]; }
        lds_w128(wbb + q * 32, __builtin_bit_cast(u32x4, v0));
        lds_w128(wbb + 128 + q * 32, __builtin_bit_cast(u32x4, v1));
        lds_w128(wbb + 8704 + q * 32, __builtin_bit_cast(u32x4, v2));
        lds_w128(wbb + 8704 + 128 + q * 32, __builtin_bit_cast(u32x4, v3));
      }
    }
    LBAR();
    {
      const int row = tid >> 3, pc = tid & 7;
      f32x4 s0 = {0, 0, 0, 0}, s1 = {0, 0, 0, 0};
#pragma unroll
      for (int q = 0; q < 8; ++q) { unsigned a = q * 17408 + row * 272 + pc * 32; s0 += __builtin_bit_cast(f32x4, lds_r128(a)); s1 += __builtin_bit_cast(f32x4, lds_r128(a + 16)); }
      const int b = 2 * bb + (row >> 5), a = row & 31; const int s = 64 * a + b;
      __builtin_amdgcn_raw_buffer_store_b128(u32x4{pk2(s0[0], s0[1]), pk2(s0[2], s0[3]), pk2(s1[0], s1[1]), pk2(s1[2], s1[3])}, __builtin_amdgcn_make_buffer_rsrc(out, 0, 2048 * 896, 0x00020000), s * 896 + ctile * 128 + pc * 16, 0, 16);
    }
  }
}
}

extern "C" void kernel_launch(void* const* d_in, const int* in_sizes, int n_in, void* d_out, int out_size, void* d_ws, size_t ws_size, hipStream_t stream) {
  using namespace gf;
  (void)hipFuncSetAttribute((const void*)fused, hipFuncAttributeMaxDynamicSharedMemorySize, LDS_BYTES);
  fused<<<256, 512, LDS_BYTES, stream>>>((const unsigned short*)d_in[0], (const unsigned short*)d_in[1], (const unsigned short*)d_in[2],
                                         (const unsigned short*)d_in[4], (const unsigned short*)d_in[5], (const unsigned short*)d_in[6],
                                         (const unsigned short*)d_in[7], (char*)d_ws, (unsigned short*)d_out);
}
```
